```python
import torch

def _softmax(x, axis=-1):
    return torch.softmax(x, dim=axis)
def _gelu(x):
    return torch.nn.functional.gelu(x, approximate="tanh")
def _silu(x):
    return torch.nn.functional.silu(x)
def _sigmoid(x):
    return torch.sigmoid(x)
def _layernorm(x, axis=-1, eps=1e-5):
    m = x.mean(dim=axis, keepdim=True)
    v = x.var(dim=axis, keepdim=True, unbiased=False)
    return (x - m) / torch.sqrt(v + eps)
def _scatter_add(tgt, idx, src, axis=0):
    return tgt.clone().index_add_(axis, idx, src)
def _segment_sum(src, seg, n):
    out = torch.zeros((n,) + src.shape[1:], dtype=src.dtype)
    return out.index_add_(0, seg, src)
def _pad(x, pw):
    flat = tuple(v for pair in reversed(pw) for v in pair)
    return torch.nn.functional.pad(x, flat)

kernel_name = 'sequential_preLN__mha_block__dense_activation_gelu__prompt'

def setup_inputs():

    _w = (lambda a: (lambda t: t.to(torch.bfloat16) if t.is_floating_point() else t)(torch.as_tensor(a)))
    def _A_setup():
        B, S, D, H = 1, 2048, 512, 32
        return {
            "x": _w((lambda *s: torch.randn(*s, dtype=torch.bfloat16) * 0.05)(B, S, D)),
            "wqkv": _w((lambda *s: torch.randn(*s, dtype=torch.bfloat16) * 0.05)(D, 3 * D)),
            "wo": _w((lambda *s: torch.randn(*s, dtype=torch.bfloat16) * 0.05)(D, D)),
        }
    _A_in = _A_setup()

    def _B_setup():
        B, S, D, F = 1, 2048, 512, 1792
        return {
            "x": _w((lambda *s: torch.randn(*s, dtype=torch.bfloat16) * 0.05)(B, S, D)),
            "w1": _w((lambda *s: torch.randn(*s, dtype=torch.bfloat16) * 0.8)(D, F) * (D ** -0.5)),
            "b1": _w((lambda *s: torch.randn(*s, dtype=torch.bfloat16) * 0.2)(F) * 0.01),
            "w2": _w((lambda *s: torch.randn(*s, dtype=torch.bfloat16) * 0.2)(F, D) * (F ** -0.5)),
            "b2": _w((lambda *s: torch.randn(*s, dtype=torch.bfloat16) * 0.2)(D) * 0.01),
        }
    _B_in = _B_setup()

    return {
        "A_x": _A_in["x"],
        "A_wqkv": _A_in["wqkv"],
        "A_wo": _A_in["wo"],
        "B_x": _B_in["x"],
        "B_w1": _B_in["w1"],
        "B_b1": _B_in["b1"],
        "B_w2": _B_in["w2"],
        "B_b2": _B_in["b2"],
    }

def reference(A_x, A_wqkv, A_wo, B_x, B_w1, B_b1, B_w2, B_b2):
    def _A_ref(x, wqkv, wo):
        B, S, D = x.shape
        H = 32
        Dh = D // H
        qkv = (lambda x, *d: x.permute(*d))((x @ wqkv).reshape(B, S, 3, H, Dh), 2, 0, 3, 1, 4)
        q, k, v = qkv[0], qkv[1], qkv[2]
        scores = torch.einsum("bhqd,bhkd->bhqk", q, k) * (Dh ** -0.5)
        p = _softmax(scores, axis=-1)
        o = torch.einsum("bhqk,bhkd->bhqd", p, v)
        return (lambda x, *d: x.permute(*d))(o, 0, 2, 1, 3).reshape(B, S, D) @ wo

    def _B_ref(x, w1, b1, w2, b2):
        h = torch.einsum("bsd,df->bsf", x, w1) + b1
        h = _gelu(h)
        return torch.einsum("bsf,fd->bsd", h, w2) + b2

    x = A_x
    h = x + _A_ref(_layernorm(x, axis=-1), A_wqkv, A_wo)
    h2 = h + _B_ref(_layernorm(h, axis=-1), B_w1, B_b1, B_w2, B_b2)
    return h2
```

```cpp
#include <hip/hip_runtime.h>
#include <hip/hip_bf16.h>
#include <stdint.h>

typedef unsigned short u16;
__device__ __forceinline__ float bf2f(u16 v){ return __uint_as_float(((unsigned)v)<<16); }
__device__ __forceinline__ u16 f2bf(float f){ unsigned u=__float_as_uint(f); u += 0x7fff + ((u>>16)&1); return (u16)(u>>16); }

constexpr int S=2048, D=512, H=32, DH=16, F=1792;

__global__ void ln_k(const u16* xb, const float* xf, float* out) {
  int r = blockIdx.x; int t = threadIdx.x;
  __shared__ float red[256];
  float v0 = xb ? bf2f(xb[r*D+t]) : xf[r*D+t];
  float v1 = xb ? bf2f(xb[r*D+t+256]) : xf[r*D+t+256];
  red[t]=v0+v1; __syncthreads();
  for (int s=128;s>0;s>>=1){ if(t<s) red[t]+=red[t+s]; __syncthreads(); }
  float m = red[0]/D; __syncthreads();
  float d0=v0-m, d1=v1-m;
  red[t]=d0*d0+d1*d1; __syncthreads();
  for (int s=128;s>0;s>>=1){ if(t<s) red[t]+=red[t+s]; __syncthreads(); }
  float rs = rsqrtf(red[0]/D + 1e-5f);
  out[r*D+t]=d0*rs; out[r*D+t+256]=d1*rs;
}
__global__ void gemm_k(const float* A, const u16* W, float* C, int N, int K, int mode, const u16* bias, const u16* rb, const float* rf, u16* ob) {
  int n = blockIdx.x*64 + (threadIdx.x&63); int m = blockIdx.y*4 + (threadIdx.x>>6);
  float acc=0;
  for (int k=0;k<K;k++) acc += A[(size_t)m*K+k]*bf2f(W[(size_t)k*N+n]);
  if (mode==1) acc += bf2f(rb[(size_t)m*N+n]);
  if (mode==2) { acc += bf2f(bias[n]); float u = 0.7978845608028654f*(acc+0.044715f*acc*acc*acc); acc = 0.5f*acc*(1.f+tanhf(u)); }
  if (mode==3) { acc += bf2f(bias[n]) + rf[(size_t)m*N+n]; ob[(size_t)m*N+n]=f2bf(acc); return; }
  C[(size_t)m*N+n]=acc;
}
__global__ void attn_k(const float* qkv, float* o) {
  int q = blockIdx.x*256+threadIdx.x; int h = blockIdx.y;
  float qv[16]; for (int d=0;d<16;d++) qv[d]=qkv[(size_t)q*1536+h*16+d]*0.25f;
  float m=-1e30f, l=0, acc[16]; for(int d=0;d<16;d++) acc[d]=0;
  for (int k=0;k<S;k++){
    const float* kp = qkv+(size_t)k*1536+512+h*16; const float* vp = qkv+(size_t)k*1536+1024+h*16;
    float s=0; for(int d=0;d<16;d++) s+=qv[d]*kp[d];
    float mn = fmaxf(m,s); float a=__expf(m-mn); float p=__expf(s-mn);
    l = l*a+p; for(int d=0;d<16;d++) acc[d]=acc[d]*a+p*vp[d]; m=mn;
  }
  for(int d=0;d<16;d++) o[(size_t)q*512+h*16+d]=acc[d]/l;
}

extern "C" void kernel_launch(void* const* d_in, const int* in_sizes, int n_in,
                              void* d_out, int out_size, void* d_ws, size_t ws_size,
                              hipStream_t stream) {
  const u16* x=(const u16*)d_in[0]; const u16* wqkv=(const u16*)d_in[1]; const u16* wo=(const u16*)d_in[2];
  const u16* w1=(const u16*)d_in[4]; const u16* b1=(const u16*)d_in[5]; const u16* w2=(const u16*)d_in[6]; const u16* b2=(const u16*)d_in[7];
  float* ws=(float*)d_ws;
  float* ln1=ws; float* qkv=ln1+S*D; float* o=qkv+S*1536; float* h=o+S*D; float* ln2=h+S*D; float* hid=ln2+S*D;
  ln_k<<<S,256,0,stream>>>(x,nullptr,ln1);
  gemm_k<<<dim3(1536/64,S/4),256,0,stream>>>(ln1,wqkv,qkv,1536,D,0,nullptr,nullptr,nullptr,nullptr);
  attn_k<<<dim3(S/256,H),256,0,stream>>>(qkv,o);
  gemm_k<<<dim3(D/64,S/4),256,0,stream>>>(o,wo,h,D,D,1,nullptr,x,nullptr,nullptr);
  ln_k<<<S,256,0,stream>>>(nullptr,h,ln2);
  gemm_k<<<dim3(F/64,S/4),256,0,stream>>>(ln2,w1,hid,F,D,2,b1,nullptr,nullptr,nullptr);
  gemm_k<<<dim3(D/64,S/4),256,0,stream>>>(hid,w2,nullptr,D,F,3,b2,nullptr,h,(u16*)d_out);
}
```

```cpp
#include <hip/hip_runtime.h>
#include <stdint.h>

typedef unsigned short u16;
typedef unsigned int u32;
typedef __attribute__((ext_vector_type(8))) __bf16 bf16x8;
typedef __attribute__((ext_vector_type(2))) __bf16 bf16x2;
typedef __attribute__((ext_vector_type(16))) float f32x16;
typedef __attribute__((ext_vector_type(4))) float f32x4;
typedef __attribute__((ext_vector_type(2))) float f32x2;
typedef __attribute__((ext_vector_type(4))) unsigned u32x4;
typedef __attribute__((ext_vector_type(2))) unsigned u32x2;
typedef __attribute__((ext_vector_type(4))) short s16x4;
#define LDS __attribute__((address_space(3)))

constexpr int S_ = 2048, D_ = 512, F_ = 1792;

constexpr size_t OFF_Q = 0;
constexpr size_t OFF_K = OFF_Q + (2u << 20);
constexpr size_t OFF_V = OFF_K + (2u << 20);
constexpr size_t OFF_O = OFF_V + (2u << 20);
constexpr size_t OFF_H = OFF_O + (2u << 20);
constexpr size_t OFF_HS = OFF_H + (2u << 20);
constexpr size_t OFF_G = OFF_HS + (1u << 20);
constexpr size_t OFF_WT = OFF_G + (8u << 20);
constexpr size_t WT_W1 = 512 * 1024;
constexpr size_t WT_W2 = WT_W1 + 1792 * 1024;

__device__ __forceinline__ u32 pk2(float a, float b) {
  bf16x2 r = {(__bf16)a, (__bf16)b};
  return __builtin_bit_cast(u32, r);
}
__device__ __forceinline__ float rbf(float a) {
  return __uint_as_float(pk2(a, a) << 16);
}
__device__ __forceinline__ float lo2f(u32 v) { return __uint_as_float(v << 16); }
__device__ __forceinline__ float hi2f(u32 v) { return __uint_as_float(v & 0xffff0000u); }
__device__ __forceinline__ f32x16 mfma(u32x4 a, u32x4 b, f32x16 c) {
  return __builtin_amdgcn_mfma_f32_32x32x16_bf16(__builtin_bit_cast(bf16x8, a), __builtin_bit_cast(bf16x8, b), c, 0, 0, 0);
}
__device__ __forceinline__ void glds16(const void* gsrc, u32 lds_dst) {
  u32 keep;
  asm volatile("s_mov_b32 %0, m0\n\ts_mov_b32 m0, %2\n\ts_nop 0\n\tglobal_load_lds_dwordx4 %1, off\n\ts_mov_b32 m0, %0"
               : "=&s"(keep) : "v"(gsrc), "s"(lds_dst) : "memory");
}
#define WAITV(n) asm volatile("s_waitcnt vmcnt(%0)" ::"i"(n) : "memory")
#define BARRIER() asm volatile("s_waitcnt lgkmcnt(0)\n\ts_barrier" ::: "memory")
__device__ __forceinline__ u32x4 ldsr128(u32 a) { return *(LDS const u32x4*)(size_t)a; }
__device__ __forceinline__ void ldsw128(u32 a, u32x4 v) { *(LDS u32x4*)(size_t)a = v; }
__device__ __forceinline__ u32x2 ldstr(u32 a) {
  return __builtin_bit_cast(u32x2, __builtin_amdgcn_ds_read_tr16_b64_v4i16((LDS s16x4*)(size_t)a));
}
__device__ __forceinline__ float dot2(u32 a, u32 b, float c) {
  return __builtin_amdgcn_fdot2_f32_bf16(__builtin_bit_cast(bf16x2, a), __builtin_bit_cast(bf16x2, b), c, false);
}
template <int CTRL>
__device__ __forceinline__ float dppadd(float v) {
  return v + __uint_as_float(__builtin_amdgcn_update_dpp(0u, __float_as_uint(v), CTRL, 0xf, 0xf, true));
}
__device__ __forceinline__ void reduce8(const float (&s)[8], float (&t)[2]) {
  float a[4];
#pragma unroll
  for (int i = 0; i < 4; i++) {
    u32x2 r = __builtin_amdgcn_permlane32_swap(__float_as_uint(s[i]), __float_as_uint(s[i + 4]), false, false);
    a[i] = __uint_as_float(r[0]) + __uint_as_float(r[1]);
  }
#pragma unroll
  for (int i = 0; i < 2; i++) {
    u32x2 r = __builtin_amdgcn_permlane16_swap(__float_as_uint(a[i]), __float_as_uint(a[i + 2]), false, false);
    float v = __uint_as_float(r[0]) + __uint_as_float(r[1]);
    v = dppadd<0x128>(v);
    v = dppadd<0x124>(v);
    v = dppadd<0x122>(v);
    v = dppadd<0x121>(v);
    t[i] = v;
  }
}
__device__ __forceinline__ float ln_rs(float var) {
  float vb = rbf(fmaxf(var, 0.f));
  float ve = rbf(vb + 1e-5f);
  float sd = rbf(__builtin_sqrtf(ve));
  return 1.0f / sd;
}

__global__ __launch_bounds__(512) void k1_qkv(const u16* __restrict__ x, const u16* __restrict__ wqkv, char* __restrict__ ws) {
  const int b = blockIdx.x, xcd = b & 7, idx = b >> 3;
  const int rb = (xcd >> 1) * 8 + (idx >> 2), quad = (xcd & 1) * 4 + (idx & 3);
  const int rot = (rb & 1) * 4;
  const int w = __builtin_amdgcn_readfirstlane(threadIdx.x >> 6), l = threadIdx.x & 63;
  constexpr u32 WB = 65536, SLOT = 24576;

  u32x4 xr[8];
  {
    const char* xp = (const char*)x + (size_t)(rb * 64 + w * 8) * 1024 + l * 16;
    asm volatile(
        "global_load_dwordx4 %0, %8, off\n\tglobal_load_dwordx4 %1, %8, off offset:1024\n\t"
        "global_load_dwordx4 %2, %8, off offset:2048\n\tglobal_load_dwordx4 %3, %8, off offset:3072\n\t"
        "global_load_dwordx4 %4, %9, off\n\tglobal_load_dwordx4 %5, %9, off offset:1024\n\t"
        "global_load_dwordx4 %6, %9, off offset:2048\n\tglobal_load_dwordx4 %7, %9, off offset:3072"
        : "=&v"(xr[0]), "=&v"(xr[1]), "=&v"(xr[2]), "=&v"(xr[3]), "=&v"(xr[4]), "=&v"(xr[5]), "=&v"(xr[6]), "=&v"(xr[7])
        : "v"(xp), "v"(xp + 4096)
        : "memory");
  }
  const int prow = l >> 3;
  const char* wl = (const char*)wqkv + (size_t)prow * 3072 + quad * 128 + (((l & 7) ^ (((prow >> 1) & 1) << 2)) << 4);
  auto issue = [&](int c, int p) {
    const int cc = (c + rot) & 7;
    glds16(wl + (size_t)(cc * 64 + (p & 7) * 8) * 3072 + (p >> 3) * 1024, WB + (c & 3) * SLOT + p * 1024);
  };
#pragma unroll
  for (int c = 0; c < 4; c++)
#pragma unroll
    for (int i = 0; i < 3; i++) issue(c, w * 3 + i);

  asm volatile("s_waitcnt vmcnt(12)"
               : "+v"(xr[0]), "+v"(xr[1]), "+v"(xr[2]), "+v"(xr[3]), "+v"(xr[4]), "+v"(xr[5]), "+v"(xr[6]), "+v"(xr[7])::"memory");
  float rs[2];
  {
    float s[8], t[2], mb[2], dm[2];
#pragma unroll
    for (int r = 0; r < 8; r++) {
      float a = dot2(xr[r][0], 0x3f803f80u, 0.f), c2 = dot2(xr[r][1], 0x3f803f80u, 0.f);
      a = dot2(xr[r][2], 0x3f803f80u, a);
      c2 = dot2(xr[r][3], 0x3f803f80u, c2);
      s[r] = a + c2;
    }
    reduce8(s, t);
#pragma unroll
    for (int i = 0; i < 2; i++) {
      float m = t[i] * (1.0f / 512.0f);
      mb[i] = rbf(m);
      dm[i] = m - mb[i];
    }
    const u32 wbase = (w >> 2) * 32768 + (l >> 1) * 1024 + (l & 1) * 512;
#pragma unroll
    for (int r = 0; r < 8; r++) {
      const float m = __uint_as_float(__builtin_amdgcn_readlane(__float_as_uint(mb[r & 1]), ((r >> 1) & 1) * 16 + (r >> 2) * 32));
      float q0 = 0.f, q1 = 0.f;
      u32x4 o;
#pragma unroll
      for (int j = 0; j < 4; j++) {
        float d0 = lo2f(xr[r][j]) - m, d1 = hi2f(xr[r][j]) - m;
        q0 = fmaf(d0, d0, q0);
        q1 = fmaf(d1, d1, q1);
        o[j] = pk2(d0, d1);
      }
      s[r] = q0 + q1;
      ldsw128(wbase + ((((w & 3) * 8 + r) ^ (l & 7)) << 4), o);
    }
    reduce8(s, t);
#pragma unroll
    for (int i = 0; i < 2; i++) rs[i] = ln_rs(t[i] * (1.0f / 512.0f) - dm[i] * dm[i]);
  }
  auto put_rs = [&]() {
    if ((l & 15) == 0) {
      const int g = l >> 4;
      const u32 a = WB + (w * 8 + 2 * (g & 1) + 4 * (g >> 1)) * 4;
      *(LDS f32x2*)(size_t)a = f32x2{rs[0], rs[1]};
    }
  };

  if (w >= 4) {
    const int lw = w - 4;
#pragma unroll
    for (int c = 0; c < 8; c++) {
      if (c == 0) WAITV(9);
      else if (c == 1) WAITV(6);
      else if (c == 2) WAITV(9);
      else if (c <= 5) WAITV(12);
      else if (c == 6) WAITV(6);
      else WAITV(0);
      BARRIER();
      if (c >= 1 && c <= 4) {
#pragma unroll
        for (int i = 0; i < 6; i++) issue(c + 3, lw * 6 + i);
      }
      if (c == 5) put_rs();
    }
    return;
  }
  const int rt = w >> 1, ct = w & 1;
  f32x16 acc[3];
#pragma unroll
  for (int s = 0; s < 3; s++)
#pragma unroll
    for (int i = 0; i < 16; i++) acc[s][i] = 0.f;
  const int g = l >> 4, q = (l >> 2) & 3, p = l & 3;
  const u32 trb = WB + (8 * (g >> 1) + q) * 128 + ((64 * ct + 32 * (g & 1) + 8 * p) ^ (64 * (q >> 1)));
  u32 xb[4];
#pragma unroll
  for (int i = 0; i < 4; i++)
    xb[i] = rt * 32768 + rot * 4096 + (l >> 5) * 512 + (((l & 31) ^ ((2 * i + (l >> 5)) & 7)) << 4);
  u32x4 xf[2], wf[2][3];
  auto rd = [&](int st, int buf) {
    const int c = st >> 2, ksl = st & 3;
    u32 xa = xb[ksl] + ((st & 15) * 1024);
    if (st >= 16) xa ^= 16384;
    xf[buf] = ldsr128(xa);
#pragma unroll
    for (int s = 0; s < 3; s++) {
      const u32 a = trb + (c & 3) * SLOT + s * 8192 + ksl * 2048;
      u32x2 lo = ldstr(a), hi = ldstr(a + 512);
      wf[buf][s] = u32x4{lo[0], lo[1], hi[0], hi[1]};
    }
  };
  WAITV(9);
  BARRIER();
  rd(0, 0);
#pragma unroll
  for (int st = 0; st < 32; st++) {
    const int nx = st + 1;
    if (nx < 32) {
      if ((nx & 3) == 0) {
        const int c = nx >> 2;
        if (c == 1) WAITV(6);
        else if (c == 2) WAITV(3);
        else if (c == 3) WAITV(0);
        BARRIER();
        if (c == 5) put_rs();
      }
      rd(nx, nx & 1);
    }
    __builtin_amdgcn_sched_barrier(0);
    acc[0] = mfma(wf[st & 1][0], xf[st & 1], acc[0]);
    acc[1] = mfma(wf[st & 1][1], xf[st & 1], acc[1]);
    acc[2] = mfma(xf[st & 1], wf[st & 1][2], acc[2]);
    __builtin_amdgcn_sched_barrier(0);
  }
  const int tile = rb * 2 + rt, hp = (quad * 2 + ct) * 2;
  const float rsl = *(LDS const float*)(size_t)(WB + (rt * 32 + (l & 31)) * 4);
  const float qs = rsl * 0.36067376022224085f;
#pragma unroll
  for (int hh = 0; hh < 2; hh++) {
    u32x4 oq, ok;
#pragma unroll
    for (int j = 0; j < 4; j++) {
      oq[j] = pk2(acc[0][hh * 8 + 2 * j] * qs, acc[0][hh * 8 + 2 * j + 1] * qs);
      ok[j] = pk2(acc[1][hh * 8 + 2 * j] * rsl, acc[1][hh * 8 + 2 * j + 1] * rsl);
    }
    const size_t o = (size_t)(hp + hh) * 65536 + tile * 1024 + l * 16;
    *(u32x4*)(ws + OFF_Q + o) = oq;
    *(u32x4*)(ws + OFF_K + o) = ok;
  }
#pragma unroll
  for (int t = 0; t < 2; t++) {
    u32x4 ov;
#pragma unroll
    for (int gg = 0; gg < 2; gg++) {
      const f32x4 r4 = *(LDS const f32x4*)(size_t)(WB + (rt * 32 + t * 16 + gg * 8 + (l >> 5) * 4) * 4);
      const int r = t * 8 + gg * 4;
      ov[gg * 2] = pk2(acc[2][r] * r4[0], acc[2][r + 1] * r4[1]);
      ov[gg * 2 + 1] = pk2(acc[2][r + 2] * r4[2], acc[2][r + 3] * r4[3]);
    }
    const size_t o = (size_t)(hp + ((l >> 4) & 1)) * 65536 + tile * 1024 + t * 512 + (l >> 5) * 256 + (l & 15) * 16;
    *(u32x4*)(ws + OFF_V + o) = ov;
  }
}

__device__ __forceinline__ void attn_tiles(u32 ka, u32 va, u32 vinc, int ngroups, const u32x4 qf, const f32x16& cm, f32x16& o) {
#pragma unroll 1
  for (int gI = 0; gI < ngroups; gI++) {
#pragma unroll
    for (int t = 0; t < 4; t++) {
      const u32x4 kf = ldsr128(ka + t * 1024);
      const u32x4 v0 = ldsr128(va + t * 1024);
      const u32x4 v1 = ldsr128(va + t * 1024 + 512);
      f32x16 s = mfma(kf, qf, cm);
      u32x4 p0, p1;
#pragma unroll
      for (int j = 0; j < 4; j++) {
        p0[j] = pk2(__builtin_amdgcn_exp2f(s[2 * j]), __builtin_amdgcn_exp2f(s[2 * j + 1]));
        p1[j] = pk2(__builtin_amdgcn_exp2f(s[8 + 2 * j]), __builtin_amdgcn_exp2f(s[8 + 2 * j + 1]));
      }
      o = mfma(v0, p0, o);
      o = mfma(v1, p1, o);
    }
    ka += 4096;
    va += vinc;
  }
}
__device__ __forceinline__ float tilemax(const f32x16& s) {
  float m = fmaxf(fmaxf(s[0], s[1]), s[2]);
#pragma unroll
  for (int i = 3; i < 15; i += 2) m = fmaxf(fmaxf(m, s[i]), s[i + 1]);
  return fmaxf(m, s[15]);
}
__device__ __forceinline__ float halfmax(float m) {
  u32x2 r = __builtin_amdgcn_permlane32_swap(__float_as_uint(m), __float_as_uint(m), false, false);
  return fmaxf(__uint_as_float(r[0]), __uint_as_float(r[1]));
}

__global__ __launch_bounds__(1024) void k2_attn(const u16* __restrict__ wo, const u16* __restrict__ w1, const u16* __restrict__ w2,
                                                char* __restrict__ ws) {
  const int b = blockIdx.x, xcd = b & 7, idx = b >> 3;
  const int head = xcd * 4 + (idx >> 3), qb = idx & 7;
  const int w = __builtin_amdgcn_readfirstlane(threadIdx.x >> 6), l = threadIdx.x & 63;
  const int qt = w & 7, kh = w >> 3;
  constexpr u32 VB = 65536, ONES = 131072, SCR = ONES + 4096;

  u32x4 qf;
  {
    const char* qp = ws + OFF_Q + (size_t)head * 65536 + (qb * 8 + qt) * 1024 + l * 16;
    asm volatile("global_load_dwordx4 %0, %1, off" : "=&v"(qf) : "v"(qp) : "memory");
  }
  {
    const int t0 = ((w >> 1) & 3) + 32 * kh;
    const char* src = ws + ((w & 1) ? OFF_V : OFF_K) + (size_t)head * 65536 + t0 * 1024 + l * 16;
    const u32 dst = (w & 1) * VB + t0 * 1024;
#pragma unroll
    for (int i = 0; i < 8; i++) glds16(src + i * 4096, dst + i * 4096);
  }
  if (threadIdx.x < 256) ldsw128(ONES + threadIdx.x * 16, u32x4{0x3f803f80u, 0x3f803f80u, 0x3f803f80u, 0x3f803f80u});
  asm volatile("s_waitcnt vmcnt(7)" : "+v"(qf)::"memory");
  BARRIER();

  const bool isv = (l & 16) == 0;
  const u32 vlane = (l >> 5) * 256 + (l & 15) * 16;
  const u32 vinc = isv ? 4096u : 0u;
  const u32 ka0 = l * 16;
  f32x16 zero;
#pragma unroll
  for (int i = 0; i < 16; i++) zero[i] = 0.f;
  f32x16 cm, o = zero;
  {
    f32x16 s = mfma(ldsr128(ka0), qf, zero);
    const float m0 = -halfmax(tilemax(s));
#pragma unroll
    for (int i = 0; i < 16; i++) cm[i] = m0;
  }
  attn_tiles(ka0 + kh * 32768, isv ? VB + kh * 32768 + vlane : ONES + vlane, vinc, 1, qf, cm, o);
  WAITV(0);
  BARRIER();
  u32 tw[8];
  const int f = b * 16 + w;
  {
    const u16* src;
    int n;
    if (f < 512) { n = 512; src = wo + (size_t)(f & 31) * 16 * 512 + (f >> 5) * 32; }
    else if (f < 2304) { const int g = f - 512; n = 1792; src = w1 + (size_t)(g & 31) * 16 * 1792 + (g >> 5) * 32; }
    else { const int g = f - 2304; n = 512; src = w2 + (size_t)(g % 112) * 16 * 512 + (g / 112) * 32; }
    src += (l >> 5) * 4 * n + (l & 31);
#pragma unroll
    for (int j = 0; j < 8; j++) tw[j] = src[(8 * (j >> 2) + (j & 3)) * n];
  }
  attn_tiles(ka0 + kh * 32768 + 4096, isv ? VB + kh * 32768 + 4096 + vlane : ONES + vlane, vinc, 1, qf, cm, o);
  *(u32x4*)(ws + OFF_WT + (size_t)f * 1024 + l * 16) =
      u32x4{tw[0] | (tw[1] << 16), tw[2] | (tw[3] << 16), tw[4] | (tw[5] << 16), tw[6] | (tw[7] << 16)};
  attn_tiles(ka0 + kh * 32768 + 8192, isv ? VB + kh * 32768 + 8192 + vlane : ONES + vlane, vinc, 6, qf, cm, o);

  const u32 sc = SCR + qt * 2560 + l * 16;
  if (kh == 1) {
    *(LDS f32x4*)(size_t)(sc) = f32x4{o[0], o[1], o[2], o[3]};
    *(LDS f32x4*)(size_t)(sc + 1024) = f32x4{o[4], o[5], o[6], o[7]};
    *(LDS float*)(size_t)(SCR + qt * 2560 + 2048 + l * 4) = o[8];
  }
  BARRIER();
  if (kh == 1) return;
  {
    const f32x4 a = *(LDS const f32x4*)(size_t)(sc), c = *(LDS const f32x4*)(size_t)(sc + 1024);
#pragma unroll
    for (int i = 0; i < 4; i++) { o[i] += a[i]; o[4 + i] += c[i]; }
    o[8] += *(LDS const float*)(size_t)(SCR + qt * 2560 + 2048 + l * 4);
  }
  float chk = fabsf(o[8]) * 0x1p28f;
#pragma unroll
  for (int i = 0; i < 8; i++) chk += fabsf(o[i]);
  if (__any(!(chk < 0x1p127f))) {
    float m = -__builtin_inff();
#pragma unroll 1
    for (int t = 0; t < 64; t++) {
      f32x16 s = mfma(ldsr128(ka0 + t * 1024), qf, zero);
      m = fmaxf(m, tilemax(s));
    }
    m = -halfmax(m);
#pragma unroll
    for (int i = 0; i < 16; i++) cm[i] = m;
    o = zero;
    attn_tiles(ka0, isv ? VB + vlane : ONES + vlane, vinc, 16, qf, cm, o);
  }
  const float il = 1.0f / o[8];
  u32x4 ov;
#pragma unroll
  for (int j = 0; j < 4; j++) ov[j] = pk2(o[2 * j] * il, o[2 * j + 1] * il);
  *(u32x4*)(ws + OFF_O + (size_t)(qb * 8 + qt) * 32768 + head * 1024 + l * 16) = ov;
}

__global__ __launch_bounds__(256) void k3_wo(const u16* __restrict__ x, char* __restrict__ ws) {
  const int b = blockIdx.x, xcd = b & 7, idx = b >> 3;
  const int rb = xcd * 4 + (idx >> 3), cb = idx & 7;
  const int w = __builtin_amdgcn_readfirstlane(threadIdx.x >> 6), l = threadIdx.x & 63;
  const int rt = w >> 1, ct = w & 1;
  {
    const char* src = (w < 2) ? ws + OFF_O + (size_t)(rb * 2 + w) * 32768 : ws + OFF_WT + (size_t)(cb * 2 + (w - 2)) * 32768;
    src += l * 16;
#pragma unroll
    for (int ks = 0; ks < 32; ks++) glds16(src + ks * 1024, ks * 4096 + w * 1024);
  }
  const int row = rb * 64 + rt * 32 + (l & 31);
  u32x2 xr[4];
  {
    const u16* xp = x + (size_t)row * 512 + cb * 64 + ct * 32 + (l >> 5) * 4;
#pragma unroll
    for (int g = 0; g < 4; g++) xr[g] = *(const u32x2*)(xp + g * 8);
  }
  f32x16 acc;
#pragma unroll
  for (int i = 0; i < 16; i++) acc[i] = 0.f;
  const u32 oa = rt * 1024 + l * 16, wa = 2048 + ct * 1024 + l * 16;
#pragma unroll
  for (int st = 0; st < 4; st++) {
    if (st == 0) WAITV(28);
    else if (st == 1) WAITV(20);
    else if (st == 2) WAITV(12);
    else WAITV(4);
    BARRIER();
#pragma unroll
    for (int k = 0; k < 8; k++) {
      const int ks = st * 8 + k;
      acc = mfma(ldsr128(wa + ks * 4096), ldsr128(oa + ks * 4096), acc);
    }
  }
  float s1 = 0.f, s2 = 0.f;
#pragma unroll
  for (int t = 0; t < 2; t++) {
    u32x4 o;
#pragma unroll
    for (int j = 0; j < 4; j++) {
      const int r = t * 8 + 2 * j;
      const u32 xv = xr[r >> 2][j & 1];
      const u32 a = pk2(acc[r], acc[r + 1]);
      const u32 h = pk2(lo2f(xv) + lo2f(a), hi2f(xv) + hi2f(a));
      o[j] = h;
      s1 = dot2(h, 0x3f803f80u, s1);
      s2 = dot2(h, h, s2);
    }
    *(u32x4*)(ws + OFF_H + (size_t)(rb * 2 + rt) * 32768 + (cb * 4 + ct * 2 + t) * 1024 + l * 16) = o;
  }
  {
    u32x2 r = __builtin_amdgcn_permlane32_swap(__float_as_uint(s1), __float_as_uint(s2), false, false);
    const float tot = __uint_as_float(r[0]) + __uint_as_float(r[1]);
    *(float*)(ws + OFF_HS + ((size_t)(cb * 2 + ct) * 2048 + row) * 8 + (l >> 5) * 4) = tot;
  }
}

__global__ __launch_bounds__(512) void k4_ffn1(const u16* __restrict__ b1, char* __restrict__ ws) {
  const int b = blockIdx.x, xcd = b & 7, idx = b >> 3;
  const int rb = xcd * 4 + (idx >> 3), sl = idx & 7;
  const int w = __builtin_amdgcn_readfirstlane(threadIdx.x >> 6), l = threadIdx.x & 63;
  constexpr u32 RS = 65536;
  u32x4 hf[8];
  const int srt = w & 1, sks = (w >> 1) * 8;
  {
    const char* hp = ws + OFF_H + (size_t)(rb * 2 + srt) * 32768 + sks * 1024 + l * 16;
#pragma unroll
    for (int i = 0; i < 8; i++) hf[i] = *(const u32x4*)(hp + i * 1024);
  }
  f32x2 st[8];
  {
    const char* sp = ws + OFF_HS + ((size_t)(l >> 5) * 8 * 2048 + rb * 64 + srt * 32 + (l & 31)) * 8;
#pragma unroll
    for (int i = 0; i < 8; i++) st[i] = *(const f32x2*)(sp + (size_t)i * 2048 * 8);
  }
  const int ct = sl * 7 + (w < 7 ? w : 6);
  u32x4 wf[32];
  {
    const char* wp = ws + OFF_WT + WT_W1 + (size_t)ct * 32768 + l * 16;
    if (w < 7) {
#pragma unroll
      for (int i = 0; i < 32; i++) wf[i] = *(const u32x4*)(wp + i * 1024);
    }
  }
  u32x2 bb[4];
#pragma unroll
  for (int g = 0; g < 4; g++) bb[g] = *(const u32x2*)(b1 + ct * 32 + g * 8 + (l >> 5) * 4);
  {
    float a1 = (st[0][0] + st[1][0]) + (st[2][0] + st[3][0]), a2 = (st[4][0] + st[5][0]) + (st[6][0] + st[7][0]);
    float q1 = (st[0][1] + st[1][1]) + (st[2][1] + st[3][1]), q2 = (st[4][1] + st[5][1]) + (st[6][1] + st[7][1]);
    double s1 = (double)a1 + (double)a2, s2 = (double)q1 + (double)q2;
    s1 += __shfl_xor(s1, 32);
    s2 += __shfl_xor(s2, 32);
    const double md = s1 * (1.0 / 512.0);
    const float var = (float)(s2 * (1.0 / 512.0) - md * md);
    const float mb = rbf((float)md);
    if (w < 2 && l < 32) *(LDS float*)(size_t)(RS + (srt * 32 + l) * 4) = ln_rs(var);
#pragma unroll
    for (int i = 0; i < 8; i++) {
      u32x4 o;
#pragma unroll
      for (int j = 0; j < 4; j++) o[j] = pk2(lo2f(hf[i][j]) - mb, hi2f(hf[i][j]) - mb);
      ldsw128(srt * 32768 + (sks + i) * 1024 + l * 16, o);
    }
  }
  BARRIER();
  if (w == 7) return;
  f32x16 acc[2];
#pragma unroll
  for (int i = 0; i < 16; i++) { acc[0][i] = 0.f; acc[1][i] = 0.f; }
#pragma unroll
  for (int ks = 0; ks < 32; ks++) {
    acc[0] = mfma(wf[ks], ldsr128(ks * 1024 + l * 16), acc[0]);
    acc[1] = mfma(wf[ks], ldsr128(32768 + ks * 1024 + l * 16), acc[1]);
  }
  float bias[16];
#pragma unroll
  for (int g = 0; g < 4; g++) {
    bias[4 * g] = lo2f(bb[g][0]); bias[4 * g + 1] = hi2f(bb[g][0]);
    bias[4 * g + 2] = lo2f(bb[g][1]); bias[4 * g + 3] = hi2f(bb[g][1]);
  }
#pragma unroll
  for (int rt = 0; rt < 2; rt++) {
    const float rs = *(LDS const float*)(size_t)(RS + (rt * 32 + (l & 31)) * 4);
    float y[16];
#pragma unroll
    for (int i = 0; i < 16; i++) {
      const float v = fmaf(acc[rt][i], rs, bias[i]);
      const float z = v * fmaf(v * v, -0.10294324f, -2.30220819f);
      y[i] = v * __builtin_amdgcn_rcpf(1.0f + __builtin_amdgcn_exp2f(z));
    }
#pragma unroll
    for (int t = 0; t < 2; t++) {
      u32x4 o;
#pragma unroll
      for (int j = 0; j < 4; j++) o[j] = pk2(y[t * 8 + 2 * j], y[t * 8 + 2 * j + 1]);
      *(u32x4*)(ws + OFF_G + (size_t)(rb * 2 + rt) * 114688 + (ct * 2 + t) * 1024 + l * 16) = o;
    }
  }
}

__global__ __launch_bounds__(512) void k5_ffn2(const u16* __restrict__ b2, const char* __restrict__ ws, u16* __restrict__ out) {
  const int b = blockIdx.x, xcd = b & 7, idx = b >> 3;
  const int rb = xcd * 4 + (idx >> 3), cb = idx & 7;
  const int w = __builtin_amdgcn_readfirstlane(threadIdx.x >> 6), l = threadIdx.x & 63;
  const char* gp = ws + OFF_G + (size_t)(rb * 2) * 114688 + w * 14336 + l * 16;
  const char* wp = ws + OFF_WT + WT_W2 + (size_t)(cb * 2) * 114688 + w * 14336 + l * 16;
  f32x16 acc[2][2];
#pragma unroll
  for (int i = 0; i < 16; i++) { acc[0][0][i] = 0.f; acc[0][1][i] = 0.f; acc[1][0][i] = 0.f; acc[1][1][i] = 0.f; }
  const int u = l >> 3, rrt = w >> 2, rrow = (w & 3) * 8 + (l & 7);
  u32x2 hr[2];
  {
    const char* hp = ws + OFF_H + (size_t)(rb * 2 + rrt) * 32768 + (cb * 4 + (u >> 1)) * 1024 + rrow * 16 + (u & 1) * 8;
    hr[0] = *(const u32x2*)hp;
    hr[1] = *(const u32x2*)(hp + 512);
  }
  const u32x4 bv = *(const u32x4*)(b2 + cb * 64 + u * 8);
  constexpr int PF = 10;
  u32x4 ga[14][2], wa[14][2];
#pragma unroll
  for (int k = 0; k < PF; k++) {
    ga[k][0] = *(const u32x4*)(gp + k * 1024);
    ga[k][1] = *(const u32x4*)(gp + 114688 + k * 1024);
    wa[k][0] = *(const u32x4*)(wp + k * 1024);
    wa[k][1] = *(const u32x4*)(wp + 114688 + k * 1024);
  }
#pragma unroll
  for (int k = 0; k < 14; k++) {
    __builtin_amdgcn_sched_barrier(0);
#pragma unroll
    for (int c = 0; c < 2; c++)
#pragma unroll
      for (int r = 0; r < 2; r++) acc[c][r] = mfma(wa[k][c], ga[k][r], acc[c][r]);
    __builtin_amdgcn_sched_barrier(0);
    if (k + PF < 14) {
      ga[k + PF][0] = *(const u32x4*)(gp + (k + PF) * 1024);
      ga[k + PF][1] = *(const u32x4*)(gp + 114688 + (k + PF) * 1024);
      wa[k + PF][0] = *(const u32x4*)(wp + (k + PF) * 1024);
      wa[k + PF][1] = *(const u32x4*)(wp + 114688 + (k + PF) * 1024);
    }
  }
  constexpr u32 WS_ = 16 * 1152;
#pragma unroll
  for (int c = 0; c < 2; c++)
#pragma unroll
    for (int r = 0; r < 2; r++)
#pragma unroll
      for (int g = 0; g < 4; g++) {
        const int uu = c * 4 + g;
        *(LDS f32x4*)(size_t)(w * WS_ + (r * 8 + uu) * 1152 + ((uu >> 1) & 1) * 128 + l * 16) =
            f32x4{acc[c][r][4 * g], acc[c][r][4 * g + 1], acc[c][r][4 * g + 2], acc[c][r][4 * g + 3]};
      }
  BARRIER();
  f32x4 s0 = {0.f, 0.f, 0.f, 0.f}, s1 = {0.f, 0.f, 0.f, 0.f};
  const u32 ra = (rrt * 8 + u) * 1152 + ((u >> 1) & 1) * 128 + rrow * 16;
#pragma unroll
  for (int i = 0; i < 8; i++) {
    s0 += *(LDS const f32x4*)(size_t)(ra + i * WS_);
    s1 += *(LDS const f32x4*)(size_t)(ra + i * WS_ + 512);
  }
  u32x4 o;
  o[0] = pk2(lo2f(hr[0][0]) + (s0[0] + lo2f(bv[0])), hi2f(hr[0][0]) + (s0[1] + hi2f(bv[0])));
  o[1] = pk2(lo2f(hr[0][1]) + (s0[2] + lo2f(bv[1])), hi2f(hr[0][1]) + (s0[3] + hi2f(bv[1])));
  o[2] = pk2(lo2f(hr[1][0]) + (s1[0] + lo2f(bv[2])), hi2f(hr[1][0]) + (s1[1] + hi2f(bv[2])));
  o[3] = pk2(lo2f(hr[1][1]) + (s1[2] + lo2f(bv[3])), hi2f(hr[1][1]) + (s1[3] + hi2f(bv[3])));
  *(u32x4*)(out + (size_t)(rb * 64 + rrt * 32 + rrow) * 512 + cb * 64 + u * 8) = o;
}

extern "C" void kernel_launch(void* const* d_in, const int* in_sizes, int n_in, void* d_out, int out_size, void* d_ws,
                              size_t ws_size, hipStream_t stream) {
  const u16* x = (const u16*)d_in[0];
  const u16* wqkv = (const u16*)d_in[1];
  const u16* wo = (const u16*)d_in[2];
  const u16* w1 = (const u16*)d_in[4];
  const u16* b1 = (const u16*)d_in[5];
  const u16* w2 = (const u16*)d_in[6];
  const u16* b2 = (const u16*)d_in[7];
  char* ws = (char*)d_ws;
  constexpr int L1 = 163840, L2 = 131072 + 4096 + 8 * 2560, L3 = 131072, L4 = 65536 + 256, L5 = 8 * 16 * 1152;
  hipFuncSetAttribute((const void*)k1_qkv, hipFuncAttributeMaxDynamicSharedMemorySize, L1);
  hipFuncSetAttribute((const void*)k2_attn, hipFuncAttributeMaxDynamicSharedMemorySize, L2);
  hipFuncSetAttribute((const void*)k3_wo, hipFuncAttributeMaxDynamicSharedMemorySize, L3);
  hipFuncSetAttribute((const void*)k4_ffn1, hipFuncAttributeMaxDynamicSharedMemorySize, L4);
  hipFuncSetAttribute((const void*)k5_ffn2, hipFuncAttributeMaxDynamicSharedMemorySize, L5);
  k1_qkv<<<256, 512, L1, stream>>>(x, wqkv, ws);
  k2_attn<<<256, 1024, L2, stream>>>(wo, w1, w2, ws);
  k3_wo<<<256, 256, L3, stream>>>(x, ws);
  k4_ffn1<<<256, 512, L4, stream>>>(b1, ws);
  k5_ffn2<<<256, 512, L5, stream>>>(b2, ws, (u16*)d_out);
}
```

```cpp
#include <hip/hip_runtime.h>
#include <stdint.h>
typedef unsigned int u32;
struct Desc { const void* p; u32 bytes; u32 cfg; };
struct Args { Desc d[9]; };

__global__ __launch_bounds__(512) void fused(Args a) {
  auto kp = __builtin_amdgcn_kernarg_segment_ptr();
  u32 bid = __builtin_amdgcn_workgroup_id_x(), tid = __builtin_amdgcn_workitem_id_x();
  asm volatile(
".Lstart:\ns_load_dwordx16 s[4:19], s[0:1], 0x0\ns_load_dwordx16 s[20:35], s[0:1], 0x40\ns_load_dwordx4 s[36:39], s[0:1], 0x80\n"
"v_lshrrev_b32 v1, 6, v0\nv_and_b32 v2, 63, v0\nv_readfirstlane_b32 s42, v1\ns_and_b32 s47, s2, 7\ns_lshr_b32 s48, s2, 3\ns_lshr_b32 s49, s47, 1\n"
"s_lshr_b32 s45, s48, 2\ns_lshl3_add_u32 s45, s49, s45\ns_and_b32 s49, s47, 1\ns_add_u32 s46, s48, 0\ns_and_b32 s46, s46, 3\n"
"s_lshl2_add_u32 s46, s49, s46\ns_lshr_b32 s43, s48, 3\ns_lshl2_add_u32 s43, s47, s43\ns_and_b32 s44, s48, 7\nv_lshlrev_b32 v87, 4, v2\n"
"s_cmp_lt_u32 s42, 4\ns_cbranch_scc1 .Lmf\ns_sub_u32 s100, s42, 4\ns_lshl_b32 s47, s100, 4\nv_lshrrev_b32 v8, 3, v2\nv_add_u32 v15, s47, v8\n"
"v_and_b32 v4, 7, v2\nv_lshrrev_b32 v5, 4, v2\nv_xor_b32 v6, v4, v5\nv_lshlrev_b32 v6, 4, v6\ns_lshl_b32 s47, s45, 16\n"
"v_lshl_add_u32 v10, v15, 10, v6\nv_add_u32 v10, s47, v10\nv_xor_b32 v11, 64, v10\nv_add_u32 v11, 0x1c00, v11\nv_and_b32 v5, 1, v5\n"
"v_lshlrev_b32 v5, 2, v5\nv_xor_b32 v6, v4, v5\nv_lshlrev_b32 v6, 4, v6\ns_and_b32 s48, s100, 1\ns_mul_i32 s48, s48, 98304\ns_lshl_b32 s47, s46, 7\n"
"s_add_u32 s47, s47, s48\nv_mul_u32_u24 v12, 0xc00, v8\nv_add3_u32 v12, v12, v6, s47\nv_add_u32 v33, 23552, v12\nv_add_u32 v34, 47104, v12\n"
"v_add_u32 v35, 70656, v12\ns_lshl_b32 s89, s100, 11\ns_lshl_b32 s90, s100, 12\ns_and_b32 s94, s46, 1\ns_mul_i32 s94, s94, 896\n"
"s_lshl_b32 s47, s45, 5\ns_and_b32 s47, s47, 128\ns_sub_u32 s95, s94, s47\ns_add_u32 s94, s94, s47\ns_add_u32 s94, s94, 1024\n"
"s_add_u32 s95, s95, 1152\ns_lshl1_add_u32 s93, s46, s45\ns_lshr_b32 s47, s100, 1\ns_add_u32 s47, s47, s93\ns_add_u32 s48, s47, 0\n"
"s_and_b32 s48, s48, 7\ns_mul_i32 s84, s48, 196608\ns_add_u32 s48, s47, 2\ns_and_b32 s48, s48, 7\ns_mul_i32 s85, s48, 196608\ns_add_u32 s48, s47, 4\n"
"s_and_b32 s48, s48, 7\ns_mul_i32 s86, s48, 196608\ns_add_u32 s48, s47, 6\ns_and_b32 s48, s48, 7\ns_mul_i32 s87, s48, 196608\ns_waitcnt lgkmcnt(0)\n"
"s_xor_b32 s1, s89, 4096\ns_add_u32 s40, s12, 0xa60000\ns_addc_u32 s41, s13, 0\ns_load_dwordx2 s[40:41], s[40:41], 0x0\ns_add_u32 m0, s89, 0\n"
"s_add_u32 s53, s93, 0\ns_and_b32 s53, s53, 7\ns_lshl_b32 s53, s53, 7\nbuffer_load_dwordx4 v10, s[4:7], s53 offen lds\n"
"buffer_load_dwordx4 v11, s[4:7], s53 offen offset:1024 lds\ns_add_u32 m0, s1, 8192\ns_add_u32 s53, s93, 1\ns_and_b32 s53, s53, 7\n"
"s_lshl_b32 s53, s53, 7\nbuffer_load_dwordx4 v10, s[4:7], s53 offen lds\nbuffer_load_dwordx4 v11, s[4:7], s53 offen offset:1024 lds\n"
"s_add_u32 s53, s84, s94\ns_add_u32 m0, s90, 65536\ns_nop 0\nbuffer_load_dwordx4 v12, s[8:11], s53 offen offset:0 lds\ns_mov_b32 s88, 0x3f803f80\n"
"v_add_u32 v13, s89, v87\nv_xor_b32 v32, 4096, v13\nv_lshlrev_b32 v14, 4, v15\nv_lshlrev_b32 v15, 2, v15\nv_mov_b32 v24, 0\nv_mov_b32 v25, 0\n"
"v_mov_b32 v26, 0\nbuffer_load_dwordx4 v33, s[8:11], s53 offen offset:1024 lds\nv_mov_b32 v27, 0\ns_cmp_eq_u32 s42, 7\n"
"s_cselect_b64 s[24:25], s[20:21], s[24:25]\ns_cselect_b64 s[26:27], s[22:23], s[26:27]\ns_cselect_b64 s[28:29], s[20:21], s[28:29]\n"
"s_cselect_b64 s[30:31], s[22:23], s[30:31]\ns_and_b32 s50, s46, 6\ns_bfe_u32 s47, s45, 0x10002\ns_or_b32 s50, s50, s47\ns_lshl_b32 s50, s50, 5\n"
"s_add_u32 s50, s45, s50\ns_lshl_b32 s50, s50, 7\nbuffer_load_dwordx4 v34, s[8:11], s53 offen offset:2048 lds\ns_and_b32 s47, s46, 1\n"
"s_mul_i32 s47, s47, 0x70000\ns_add_u32 s50, s50, s47\ns_add_u32 s50, s50, 0xa00000\ns_lshl_b32 s51, s44, 5\ns_add_u32 s51, s43, s51\n"
"s_lshl_b32 s51, s51, 7\ns_add_u32 s51, s51, 0xa10000\nbuffer_load_dwordx4 v35, s[8:11], s53 offen offset:3072 lds\ns_lshl_b32 s48, s42, 10\n"
"s_lshl_b32 s49, s43, 16\ns_add_u32 s49, s49, s48\ns_add_u32 s49, s49, 0x200000\ns_and_b32 s56, s42, 3\ns_lshr_b32 s57, s42, 2\ns_mul_i32 s58, s44, 7\n"
"s_add_u32 s58, s58, s42\ns_add_u32 m0, s89, 16384\ns_add_u32 s53, s93, 2\ns_and_b32 s53, s53, 7\ns_lshl_b32 s53, s53, 7\n"
"buffer_load_dwordx4 v10, s[4:7], s53 offen lds\ns_lshl_b32 s21, s58, 6\ns_lshl_b32 s58, s58, 15\ns_add_u32 s58, s58, 0x1200000\n"
"s_lshl3_add_u32 s59, s44, s42\ns_mul_i32 s59, s59, 28672\ns_add_u32 s59, s59, 0x1400000\ns_lshl1_add_u32 s60, s56, s57\n"
"s_lshl3_add_u32 s60, s44, s60\nbuffer_load_dwordx4 v11, s[4:7], s53 offen offset:1024 lds\ns_lshl_b32 s60, s60, 13\ns_add_u32 s60, s60, 0x1600000\n"
"s_and_b32 s61, s56, 1\ns_lshl_b32 s61, s61, 14\ns_lshr_b32 s47, s56, 1\ns_lshl_b32 s47, s47, 16\ns_add_u32 s61, s61, s47\ns_mul_i32 s48, s43, 57344\n"
"s_add_u32 m0, s1, 24576\ns_add_u32 s53, s93, 3\ns_and_b32 s53, s53, 7\ns_lshl_b32 s53, s53, 7\nbuffer_load_dwordx4 v10, s[4:7], s53 offen lds\n"
"s_add_u32 s21, s21, s48\ns_lshl_b32 s47, s43, 10\ns_add_u32 s54, s47, s58\ns_and_b32 s47, s43, 3\ns_mul_i32 s47, s47, 7\ns_add_u32 s47, s47, s42\n"
"s_lshr_b32 s48, s43, 2\ns_mul_i32 s22, s48, 14\nbuffer_load_dwordx4 v11, s[4:7], s53 offen offset:1024 lds\ns_lshr_b32 s0, s47, 1\n"
"s_add_u32 s22, s22, s0\ns_lshl_b32 s22, s22, 14\ns_lshr_b32 s0, s48, 1\ns_xor_b32 s0, s0, s47\ns_and_b32 s0, s0, 1\ns_lshl_b32 s0, s0, 6\n"
"s_add_u32 s22, s22, s0\ns_add_u32 m0, s89, 32768\ns_add_u32 s53, s93, 4\ns_and_b32 s53, s53, 7\ns_lshl_b32 s53, s53, 7\n"
"buffer_load_dwordx4 v10, s[4:7], s53 offen lds\ns_lshl_b32 s0, s44, 7\ns_add_u32 s22, s22, s0\ns_and_b32 s52, s48, 1\ns_lshr_b32 s48, s48, 2\n"
"s_lshl1_add_u32 s52, s52, s48\ns_lshl_b32 s52, s52, 4\ns_mul_i32 s47, s44, 224\ns_mul_i32 s48, s43, 7\n"
"buffer_load_dwordx4 v11, s[4:7], s53 offen offset:1024 lds\ns_add_u32 s47, s47, s48\ns_add_u32 s47, s47, s42\ns_lshl_b32 s47, s47, 10\n"
"s_add_u32 s55, s47, 0x1400000\ns_lshl_b32 s47, s43, 14\ns_add_u32 s0, s0, s47\ns_lshr_b32 s48, s43, 4\ns_lshl_b32 s48, s48, 6\n"
"s_add_u32 m0, s1, 40960\ns_add_u32 s53, s93, 5\ns_and_b32 s53, s53, 7\ns_lshl_b32 s53, s53, 7\nbuffer_load_dwordx4 v10, s[4:7], s53 offen lds\n"
"s_add_u32 s0, s0, s48\ns_xor_b32 s101, s0, 64\ns_lshl_b32 s3, s43, 2\ns_and_b32 s3, s3, 32\ns_lshl_b32 s47, s44, 16\ns_lshl_b32 s48, s43, 11\n"
"s_add_u32 s47, s47, s48\ns_add_u32 s47, s47, 0x1600000\nbuffer_load_dwordx4 v11, s[4:7], s53 offen offset:1024 lds\ns_add_u32 s48, s47, 0x400\n"
"s_movk_i32 s23, 0xe00\ns_cmp_eq_u32 s42, 7\ns_cselect_b32 s54, s47, s54\ns_cselect_b32 s55, s48, s55\ns_cselect_b32 s21, s0, s21\n"
"s_cselect_b32 s22, s101, s22\ns_cselect_b32 s23, 0x400, s23\ns_cselect_b32 s52, s3, s52\ns_cselect_b32 s3, s3, 0\ns_lshl_b32 s20, s42, 11\n"
"s_add_u32 s20, s20, 0x21000\nv_lshrrev_b32 v4, 2, v2\nv_and_b32 v5, 3, v2\nv_lshlrev_b32 v5, 4, v5\ns_add_u32 s53, s85, s94\n"
"s_add_u32 m0, s90, 81920\ns_nop 0\nbuffer_load_dwordx4 v12, s[8:11], s53 offen offset:0 lds\nv_xor_b32 v6, s3, v5\nv_mad_u32_u24 v100, v4, s23, v6\n"
"v_xor_b32 v6, s52, v5\nv_lshl_add_u32 v101, v4, 10, v6\nv_lshrrev_b32 v4, 5, v2\nv_bfe_u32 v5, v2, 2, 2\nv_lshl_add_u32 v6, v4, 2, v5\n"
"v_lshlrev_b32 v6, 6, v6\nbuffer_load_dwordx4 v33, s[8:11], s53 offen offset:1024 lds\nv_bfe_u32 v5, v2, 4, 1\nv_lshl_add_u32 v6, v5, 5, v6\n"
"v_and_b32 v3, 3, v2\nv_lshl_add_u32 v6, v3, 3, v6\nv_add_u32 v102, s20, v6\nv_and_b32 v3, 15, v2\nv_lshlrev_b32 v3, 4, v3\n"
"v_lshl_add_u32 v3, v4, 8, v3\nbuffer_load_dwordx4 v34, s[8:11], s53 offen offset:2048 lds\nv_add_u32 v6, 1, v5\nv_lshl_add_u32 v72, v6, 16, v3\n"
"v_xor_b32 v5, 1, v5\nv_lshlrev_b32 v73, 12, v5\ns_lshl_b32 s48, s42, 10\nv_add_u32 v74, s48, v87\nv_add_u32 v75, 0x10000, v74\nv_and_b32 v3, 7, v2\n"
"buffer_load_dwordx4 v35, s[8:11], s53 offen offset:3072 lds\nv_bfe_u32 v4, v2, 3, 1\nv_mul_u32_u24 v4, 0x70000, v4\nv_lshl_add_u32 v3, v3, 9, v4\n"
"s_lshr_b32 s47, s42, 1\ns_lshl_b32 s47, s47, 7\ns_lshr_b32 s48, s43, 2\ns_lshl_b32 s48, s48, 12\ns_add_u32 s47, s47, s48\n"
"s_waitcnt vmcnt(12) lgkmcnt(0)\ns_barrier\ns_add_u32 s40, s40, 0x9e3779b9\ns_xor_b32 s41, s41, 0x85ebca6b\nds_read_b128 v[128:131], v13 offset:0\n"
"ds_read_b128 v[132:135], v13 offset:1024\nds_read_b128 v[136:139], v32 offset:8192\nds_read_b128 v[140:143], v32 offset:9216\n"
"s_add_u32 m0, s89, 49152\ns_add_u32 s53, s93, 6\ns_and_b32 s53, s53, 7\ns_lshl_b32 s53, s53, 7\nbuffer_load_dwordx4 v10, s[4:7], s53 offen lds\n"
"s_add_u32 s47, s47, 0xa00000\nv_add_u32 v78, s47, v3\ns_cmp_eq_u32 s42, 7\ns_cselect_b32 s47, 0x40000000, 0\nv_add_u32 v79, s47, v87\n"
"v_and_b32 v3, 31, v2\ns_lshl_b32 s47, s44, 12\ns_add_u32 s47, s47, 0xa10000\nv_lshl_add_u32 v86, v3, 7, s47\n"
"buffer_load_dwordx4 v11, s[4:7], s53 offen offset:1024 lds\ns_lshl_b32 s47, s42, 2\ns_and_b32 s47, s47, 24\nv_mov_b32 v84, s47\nv_and_b32 v3, 22, v2\n"
"v_cmp_eq_u32 vcc, 0, v3\nv_mov_b32 v4, 0x3f803f80\ns_nop 0\nv_cndmask_b32 v4, 0, v4, vcc\nv_mov_b32 v5, v4\ns_add_u32 m0, s1, 57344\n"
"s_add_u32 s53, s93, 7\ns_and_b32 s53, s53, 7\ns_lshl_b32 s53, s53, 7\nbuffer_load_dwordx4 v10, s[4:7], s53 offen lds\ns_lshl_b32 s47, s100, 9\n"
"s_add_u32 s47, s47, 0x20000\nv_lshl_add_u32 v3, v2, 3, s47\nv_mov_b32 v48, 0\nv_mov_b32 v49, 0\nv_mov_b32 v50, 0\nv_mov_b32 v51, 0\n"
"v_mov_b32 v8, 0x27000\nbuffer_load_dwordx4 v11, s[4:7], s53 offen offset:1024 lds\ns_lshr_b32 s47, s42, 1\ns_sub_u32 s47, s100, s47\n"
"s_lshl_b32 s47, s47, 7\nv_add_u32 v36, s47, v78\nv_add_u32 v37, 0x10000, v13\ns_lshl3_add_u32 s52, s43, s44\ns_lshl2_add_u32 s52, s52, s100\n"
"s_lshl_b32 s52, s52, 12\ns_add_u32 s53, s86, s94\ns_add_u32 m0, s90, 98304\ns_nop 0\nbuffer_load_dwordx4 v12, s[8:11], s53 offen offset:0 lds\n"
"s_add_u32 s52, s52, 0x1800000\ns_lshl_b32 s47, s100, 3\nv_mov_b32 v38, s47\ns_waitcnt lgkmcnt(3)\nv_dot2c_f32_bf16 v24, s88, v128\n"
"v_dot2c_f32_bf16 v25, v128, v128\nv_dot2c_f32_bf16 v24, s88, v129\nv_dot2c_f32_bf16 v25, v129, v129\nv_dot2c_f32_bf16 v24, s88, v130\n"
"buffer_load_dwordx4 v33, s[8:11], s53 offen offset:1024 lds\nv_dot2c_f32_bf16 v25, v130, v130\nv_dot2c_f32_bf16 v24, s88, v131\n"
"v_dot2c_f32_bf16 v25, v131, v131\ns_waitcnt lgkmcnt(2)\nv_dot2c_f32_bf16 v26, s88, v132\nv_dot2c_f32_bf16 v27, v132, v132\n"
"v_dot2c_f32_bf16 v26, s88, v133\nv_dot2c_f32_bf16 v27, v133, v133\nv_dot2c_f32_bf16 v26, s88, v134\ns_waitcnt vmcnt(16)\n"
"ds_read_b128 v[144:147], v13 offset:16384\nds_read_b128 v[148:151], v13 offset:17408\nbuffer_load_dwordx4 v34, s[8:11], s53 offen offset:2048 lds\n"
"v_dot2c_f32_bf16 v27, v134, v134\nv_dot2c_f32_bf16 v26, s88, v135\nv_dot2c_f32_bf16 v27, v135, v135\ns_waitcnt lgkmcnt(3)\n"
"v_dot2c_f32_bf16 v24, s88, v136\nv_dot2c_f32_bf16 v25, v136, v136\nv_dot2c_f32_bf16 v24, s88, v137\nv_dot2c_f32_bf16 v25, v137, v137\n"
"v_dot2c_f32_bf16 v24, s88, v138\nbuffer_load_dwordx4 v35, s[8:11], s53 offen offset:3072 lds\nv_dot2c_f32_bf16 v25, v138, v138\n"
"v_dot2c_f32_bf16 v24, s88, v139\nv_dot2c_f32_bf16 v25, v139, v139\ns_waitcnt lgkmcnt(2)\nv_dot2c_f32_bf16 v26, s88, v140\n"
"v_dot2c_f32_bf16 v27, v140, v140\nv_dot2c_f32_bf16 v26, s88, v141\nv_dot2c_f32_bf16 v27, v141, v141\nv_dot2c_f32_bf16 v26, s88, v142\n"
"s_waitcnt vmcnt(16)\nds_read_b128 v[152:155], v32 offset:24576\nds_read_b128 v[156:159], v32 offset:25600\ns_add_u32 s53, s87, s94\n"
"s_add_u32 m0, s90, 114688\ns_nop 0\nbuffer_load_dwordx4 v12, s[8:11], s53 offen offset:0 lds\nv_dot2c_f32_bf16 v27, v142, v142\n"
"v_dot2c_f32_bf16 v26, s88, v143\nv_dot2c_f32_bf16 v27, v143, v143\ns_waitcnt lgkmcnt(3)\nv_dot2c_f32_bf16 v24, s88, v144\n"
"v_dot2c_f32_bf16 v25, v144, v144\nv_dot2c_f32_bf16 v24, s88, v145\nv_dot2c_f32_bf16 v25, v145, v145\nv_dot2c_f32_bf16 v24, s88, v146\n"
"buffer_load_dwordx4 v33, s[8:11], s53 offen offset:1024 lds\nv_dot2c_f32_bf16 v25, v146, v146\nv_dot2c_f32_bf16 v24, s88, v147\n"
"v_dot2c_f32_bf16 v25, v147, v147\ns_waitcnt lgkmcnt(2)\nv_dot2c_f32_bf16 v26, s88, v148\nv_dot2c_f32_bf16 v27, v148, v148\n"
"v_dot2c_f32_bf16 v26, s88, v149\nv_dot2c_f32_bf16 v27, v149, v149\nv_dot2c_f32_bf16 v26, s88, v150\ns_waitcnt vmcnt(16)\n"
"ds_read_b128 v[160:163], v13 offset:32768\nds_read_b128 v[164:167], v13 offset:33792\nbuffer_load_dwordx4 v34, s[8:11], s53 offen offset:2048 lds\n"
"v_dot2c_f32_bf16 v27, v150, v150\nv_dot2c_f32_bf16 v26, s88, v151\nv_dot2c_f32_bf16 v27, v151, v151\ns_waitcnt lgkmcnt(3)\n"
"v_dot2c_f32_bf16 v24, s88, v152\nv_dot2c_f32_bf16 v25, v152, v152\nv_dot2c_f32_bf16 v24, s88, v153\nv_dot2c_f32_bf16 v25, v153, v153\n"
"v_dot2c_f32_bf16 v24, s88, v154\nbuffer_load_dwordx4 v35, s[8:11], s53 offen offset:3072 lds\nv_dot2c_f32_bf16 v25, v154, v154\n"
"v_dot2c_f32_bf16 v24, s88, v155\nv_dot2c_f32_bf16 v25, v155, v155\ns_waitcnt lgkmcnt(2)\nv_dot2c_f32_bf16 v26, s88, v156\n"
"v_dot2c_f32_bf16 v27, v156, v156\nv_dot2c_f32_bf16 v26, s88, v157\nv_dot2c_f32_bf16 v27, v157, v157\nv_dot2c_f32_bf16 v26, s88, v158\n"
"s_waitcnt vmcnt(16)\nds_read_b128 v[168:171], v32 offset:40960\nds_read_b128 v[172:175], v32 offset:41984\ns_add_u32 m0, s90, 131072\ns_nop 0\n"
"buffer_load_dwordx4 v12, s[8:11], s84 offen offset:0 lds\nv_dot2c_f32_bf16 v27, v158, v158\nv_dot2c_f32_bf16 v26, s88, v159\n"
"v_dot2c_f32_bf16 v27, v159, v159\ns_waitcnt lgkmcnt(3)\nv_dot2c_f32_bf16 v24, s88, v160\nv_dot2c_f32_bf16 v25, v160, v160\n"
"v_dot2c_f32_bf16 v24, s88, v161\nv_dot2c_f32_bf16 v25, v161, v161\nv_dot2c_f32_bf16 v24, s88, v162\n"
"buffer_load_dwordx4 v33, s[8:11], s84 offen offset:1024 lds\nv_dot2c_f32_bf16 v25, v162, v162\nv_dot2c_f32_bf16 v24, s88, v163\n"
"v_dot2c_f32_bf16 v25, v163, v163\ns_waitcnt lgkmcnt(2)\nv_dot2c_f32_bf16 v26, s88, v164\nv_dot2c_f32_bf16 v27, v164, v164\n"
"v_dot2c_f32_bf16 v26, s88, v165\nv_dot2c_f32_bf16 v27, v165, v165\nv_dot2c_f32_bf16 v26, s88, v166\n"
"buffer_load_dwordx4 v34, s[8:11], s84 offen offset:2048 lds\nv_dot2c_f32_bf16 v27, v166, v166\nv_dot2c_f32_bf16 v26, s88, v167\n"
"v_dot2c_f32_bf16 v27, v167, v167\ns_waitcnt lgkmcnt(1)\nv_dot2c_f32_bf16 v24, s88, v168\nv_dot2c_f32_bf16 v25, v168, v168\n"
"v_dot2c_f32_bf16 v24, s88, v169\nv_dot2c_f32_bf16 v25, v169, v169\nv_dot2c_f32_bf16 v24, s88, v170\n"
"buffer_load_dwordx4 v35, s[8:11], s84 offen offset:3072 lds\nv_dot2c_f32_bf16 v25, v170, v170\nv_dot2c_f32_bf16 v24, s88, v171\n"
"v_dot2c_f32_bf16 v25, v171, v171\ns_waitcnt lgkmcnt(0)\nv_dot2c_f32_bf16 v26, s88, v172\nv_dot2c_f32_bf16 v27, v172, v172\n"
"v_dot2c_f32_bf16 v26, s88, v173\nv_dot2c_f32_bf16 v27, v173, v173\nv_dot2c_f32_bf16 v26, s88, v174\ns_waitcnt vmcnt(16) lgkmcnt(0)\ns_barrier\n"
"s_add_u32 m0, s90, 147456\ns_nop 0\nbuffer_load_dwordx4 v12, s[8:11], s85 offen offset:0 lds\nv_dot2c_f32_bf16 v27, v174, v174\n"
"v_dot2c_f32_bf16 v26, s88, v175\nv_dot2c_f32_bf16 v27, v175, v175\nbuffer_load_dwordx4 v33, s[8:11], s85 offen offset:1024 lds\ns_waitcnt vmcnt(16)\n"
"ds_read_b128 v[176:179], v13 offset:49152\nds_read_b128 v[180:183], v13 offset:50176\nbuffer_load_dwordx4 v34, s[8:11], s85 offen offset:2048 lds\n"
"s_waitcnt lgkmcnt(1)\nv_dot2c_f32_bf16 v24, s88, v176\nv_dot2c_f32_bf16 v25, v176, v176\nv_dot2c_f32_bf16 v24, s88, v177\n"
"v_dot2c_f32_bf16 v25, v177, v177\nv_dot2c_f32_bf16 v24, s88, v178\nv_dot2c_f32_bf16 v25, v178, v178\nv_dot2c_f32_bf16 v24, s88, v179\n"
"v_dot2c_f32_bf16 v25, v179, v179\nbuffer_load_dwordx4 v35, s[8:11], s85 offen offset:3072 lds\ns_waitcnt lgkmcnt(0)\nv_dot2c_f32_bf16 v26, s88, v180\n"
"v_dot2c_f32_bf16 v27, v180, v180\nv_dot2c_f32_bf16 v26, s88, v181\nv_dot2c_f32_bf16 v27, v181, v181\nv_dot2c_f32_bf16 v26, s88, v182\n"
"v_dot2c_f32_bf16 v27, v182, v182\nv_dot2c_f32_bf16 v26, s88, v183\nv_dot2c_f32_bf16 v27, v183, v183\ns_waitcnt vmcnt(16)\n"
"ds_read_b128 v[184:187], v32 offset:57344\nds_read_b128 v[188:191], v32 offset:58368\ns_add_u32 m0, s90, 65536\ns_nop 0\n"
"buffer_load_dwordx4 v12, s[8:11], s86 offen offset:0 lds\ns_waitcnt lgkmcnt(1)\nv_dot2c_f32_bf16 v24, s88, v184\nv_dot2c_f32_bf16 v25, v184, v184\n"
"v_dot2c_f32_bf16 v24, s88, v185\nv_dot2c_f32_bf16 v25, v185, v185\nv_dot2c_f32_bf16 v24, s88, v186\nv_dot2c_f32_bf16 v25, v186, v186\n"
"v_dot2c_f32_bf16 v24, s88, v187\nv_dot2c_f32_bf16 v25, v187, v187\nbuffer_load_dwordx4 v33, s[8:11], s86 offen offset:1024 lds\ns_waitcnt lgkmcnt(0)\n"
"v_dot2c_f32_bf16 v26, s88, v188\nv_dot2c_f32_bf16 v27, v188, v188\nv_dot2c_f32_bf16 v26, s88, v189\nv_dot2c_f32_bf16 v27, v189, v189\n"
"v_dot2c_f32_bf16 v26, s88, v190\nv_dot2c_f32_bf16 v27, v190, v190\nv_dot2c_f32_bf16 v26, s88, v191\nv_dot2c_f32_bf16 v27, v191, v191\n"
"buffer_load_dwordx4 v34, s[8:11], s86 offen offset:2048 lds\nv_add_f32_dpp v24, v24, v24 quad_perm:[1,0,3,2] row_mask:0xf bank_mask:0xf\n"
"v_add_f32_dpp v25, v25, v25 quad_perm:[1,0,3,2] row_mask:0xf bank_mask:0xf\n"
"v_add_f32_dpp v26, v26, v26 quad_perm:[1,0,3,2] row_mask:0xf bank_mask:0xf\n"
"v_add_f32_dpp v27, v27, v27 quad_perm:[1,0,3,2] row_mask:0xf bank_mask:0xf\n"
"v_add_f32_dpp v24, v24, v24 quad_perm:[2,3,0,1] row_mask:0xf bank_mask:0xf\n"
"v_add_f32_dpp v25, v25, v25 quad_perm:[2,3,0,1] row_mask:0xf bank_mask:0xf\n"
"v_add_f32_dpp v26, v26, v26 quad_perm:[2,3,0,1] row_mask:0xf bank_mask:0xf\n"
"v_add_f32_dpp v27, v27, v27 quad_perm:[2,3,0,1] row_mask:0xf bank_mask:0xf\nbuffer_load_dwordx4 v35, s[8:11], s86 offen offset:3072 lds\n"
"v_add_f32_dpp v24, v24, v24 row_half_mirror row_mask:0xf bank_mask:0xf\nv_add_f32_dpp v25, v25, v25 row_half_mirror row_mask:0xf bank_mask:0xf\n"
"v_add_f32_dpp v26, v26, v26 row_half_mirror row_mask:0xf bank_mask:0xf\nv_add_f32_dpp v27, v27, v27 row_half_mirror row_mask:0xf bank_mask:0xf\n"
"v_mul_f32 v24, 0x3b000000, v24\nv_mul_f32 v26, 0x3b000000, v26\nv_mul_f32 v25, 0x3b000000, v25\nv_mul_f32 v27, 0x3b000000, v27\n"
"s_waitcnt vmcnt(16) lgkmcnt(0)\ns_barrier\ns_add_u32 m0, s90, 0\ns_nop 0\nbuffer_load_dwordx4 v12, s[8:11], s87 offen offset:0 lds\n"
"v_fma_f32 v25, -v24, v24, v25\nv_fma_f32 v27, -v26, v26, v27\nv_max_f32 v25, 0, v25\nv_max_f32 v27, 0, v27\nv_cvt_pk_bf16_f32 v25, v25, v25\n"
"v_cvt_pk_bf16_f32 v27, v27, v27\nv_lshlrev_b32 v25, 16, v25\nv_lshlrev_b32 v27, 16, v27\nbuffer_load_dwordx4 v33, s[8:11], s87 offen offset:1024 lds\n"
"v_add_f32 v25, 0x3727c5ac, v25\nv_add_f32 v27, 0x3727c5ac, v27\nv_cvt_pk_bf16_f32 v25, v25, v25\nv_cvt_pk_bf16_f32 v27, v27, v27\n"
"v_lshlrev_b32 v25, 16, v25\nv_lshlrev_b32 v27, 16, v27\nv_sqrt_f32 v25, v25\nv_sqrt_f32 v27, v27\n"
"buffer_load_dwordx4 v34, s[8:11], s87 offen offset:2048 lds\nv_cvt_pk_bf16_f32 v16, -v24, -v24\nv_cvt_pk_bf16_f32 v20, -v26, -v26\n"
"v_cvt_pk_bf16_f32 v25, v25, v25\nv_cvt_pk_bf16_f32 v27, v27, v27\nv_lshlrev_b32 v25, 16, v25\nv_lshlrev_b32 v27, 16, v27\nv_rcp_f32 v18, v25\n"
"v_rcp_f32 v22, v27\nbuffer_load_dwordx4 v35, s[8:11], s87 offen offset:3072 lds\nv_mov_b32 v17, v16\nv_mov_b32 v21, v20\n"
"v_mul_f32 v19, 0x3eb8aa3b, v18\nv_mul_f32 v23, 0x3eb8aa3b, v22\ns_waitcnt vmcnt(16) lgkmcnt(0)\ns_barrier\ns_add_u32 s53, s84, s95\n"
"s_add_u32 m0, s90, 81920\ns_nop 0\nbuffer_load_dwordx4 v12, s[8:11], s53 offen offset:0 lds\n"
"buffer_load_dwordx4 v33, s[8:11], s53 offen offset:1024 lds\nbuffer_load_dwordx4 v34, s[8:11], s53 offen offset:2048 lds\n"
"buffer_load_dwordx4 v35, s[8:11], s53 offen offset:3072 lds\nds_write_b128 v14, v[16:19] offset:16384\nds_write_b32 v15, v18 offset:17408\n"
"ds_write_b128 v14, v[20:23] offset:16512\nds_write_b32 v15, v22 offset:17440\ns_waitcnt vmcnt(16) lgkmcnt(0)\ns_barrier\ns_add_u32 s53, s85, s95\n"
"s_add_u32 m0, s90, 98304\ns_nop 0\nbuffer_load_dwordx4 v12, s[8:11], s53 offen offset:0 lds\n"
"buffer_load_dwordx4 v33, s[8:11], s53 offen offset:1024 lds\nbuffer_load_dwordx4 v34, s[8:11], s53 offen offset:2048 lds\n"
"buffer_load_dwordx4 v35, s[8:11], s53 offen offset:3072 lds\ns_waitcnt vmcnt(16) lgkmcnt(0)\ns_barrier\ns_add_u32 s53, s86, s95\n"
"s_add_u32 m0, s90, 114688\ns_nop 0\nbuffer_load_dwordx4 v12, s[8:11], s53 offen offset:0 lds\n"
"buffer_load_dwordx4 v33, s[8:11], s53 offen offset:1024 lds\nbuffer_load_dwordx4 v34, s[8:11], s53 offen offset:2048 lds\n"
"buffer_load_dwordx4 v35, s[8:11], s53 offen offset:3072 lds\ns_waitcnt vmcnt(16) lgkmcnt(0)\ns_barrier\ns_add_u32 s53, s87, s95\n"
"s_add_u32 m0, s90, 49152\ns_nop 0\nbuffer_load_dwordx4 v12, s[8:11], s53 offen offset:0 lds\n"
"buffer_load_dwordx4 v33, s[8:11], s53 offen offset:1024 lds\nbuffer_load_dwordx4 v34, s[8:11], s53 offen offset:2048 lds\n"
"buffer_load_dwordx4 v35, s[8:11], s53 offen offset:3072 lds\ns_waitcnt vmcnt(16) lgkmcnt(0)\ns_barrier\ns_cmp_lg_u32 s42, 4\ns_cbranch_scc1 .Lf0\n"
"v_mov_b32 v16, s40\nv_mov_b32 v17, s41\ns_mov_b64 exec, 1\nbuffer_store_dwordx2 v[16:17], off, s[12:15], s50 sc1\ns_mov_b64 exec, -1\n.Lf0:\n"
"ds_write_b64 v3, v[4:5]\nds_write_b64 v3, v[4:5] offset:2048\ns_waitcnt vmcnt(12) lgkmcnt(0)\ns_barrier\n"
"buffer_load_dwordx2 v[76:77], v36, s[12:15], 0 offen sc1\ns_waitcnt vmcnt(1) lgkmcnt(0)\ns_barrier\ns_lshl_b32 s0, s43, 16\ns_add_u32 s0, s0, s89\n"
"s_add_u32 s0, s0, 0x200000\ns_bfe_u32 s3, s43, 0x10002\ns_mov_b32 s48, 0\ns_branch .Lp0_c\n.Lp0:\ns_waitcnt vmcnt(0)\n"
"buffer_load_dwordx2 v[76:77], v36, s[12:15], 0 offen sc1\n.Lp0_c:\ns_waitcnt vmcnt(0)\nv_cmp_eq_u64 vcc, s[40:41], v[76:77]\ns_add_u32 s48, s48, 1\n"
"s_cmp_lt_u32 s48, 0x40000\ns_cselect_b32 s100, vcc_lo, -1\ns_lshr_b32 s47, s100, 8\ns_and_b32 s100, s100, s47\ns_bitcmp1_b32 s100, s3\n"
"s_cbranch_scc0 .Lp0\ns_lshl_b32 s47, s3, 13\ns_add_u32 s3, s3, 2\ns_add_u32 s47, s47, s0\ns_add_u32 s53, s47, 0x200000\n"
"buffer_load_dwordx4 v[128:131], v87, s[12:15], s47 offen offset:0 sc1\nbuffer_load_dwordx4 v[132:135], v87, s[12:15], s53 offen offset:0 sc1\n"
"buffer_load_dwordx4 v[136:139], v87, s[12:15], s47 offen offset:1024 sc1\nbuffer_load_dwordx4 v[140:143], v87, s[12:15], s53 offen offset:1024 sc1\n"
"buffer_load_dwordx4 v[144:147], v87, s[12:15], s52 offen offset:0 sc1\nbuffer_load_dwordx4 v[148:151], v87, s[12:15], s52 offen offset:1024 sc1\n"
"buffer_load_dwordx4 v[152:155], v87, s[12:15], s52 offen offset:2048 sc1\nbuffer_load_dwordx4 v[156:159], v87, s[12:15], s52 offen offset:3072 sc1\n"
"buffer_load_dwordx2 v[76:77], v36, s[12:15], 0 offen sc1\ns_mov_b32 s98, s40\ns_mov_b32 s99, s40\ns_mov_b32 s48, 0\nv_mov_b32 v17, s3\n"
"s_waitcnt vmcnt(8)\nds_write_b128 v13, v[128:131] offset:0\ns_waitcnt vmcnt(7)\nds_write_b128 v37, v[132:135] offset:0\ns_waitcnt vmcnt(6)\n"
"ds_write_b128 v13, v[136:139] offset:1024\ns_waitcnt vmcnt(5)\nds_write_b128 v37, v[140:143] offset:1024\n.Lpq:\ns_waitcnt vmcnt(0)\n"
"v_min3_u32 v0, v145, v147, v149\nv_min3_u32 v2, v151, v153, v155\nv_max3_u32 v1, v145, v147, v149\nv_max3_u32 v3, v151, v153, v155\n"
"v_min3_u32 v0, v0, v157, v159\nv_min_u32 v0, v0, v2\nv_max3_u32 v1, v1, v157, v159\nv_max_u32 v1, v1, v3\nv_cmp_ne_u64 vcc, s[98:99], v[0:1]\n"
"s_add_u32 s48, s48, 1\ns_cmp_lt_u32 s48, 0x40000\ns_cselect_b64 vcc, vcc, 0\ns_cbranch_vccz .Lpq_ok\n"
"buffer_load_dwordx4 v[144:147], v87, s[12:15], s52 offen offset:0 sc1\nbuffer_load_dwordx4 v[148:151], v87, s[12:15], s52 offen offset:1024 sc1\n"
"buffer_load_dwordx4 v[152:155], v87, s[12:15], s52 offen offset:2048 sc1\nbuffer_load_dwordx4 v[156:159], v87, s[12:15], s52 offen offset:3072 sc1\n"
"buffer_load_dwordx2 v[76:77], v36, s[12:15], 0 offen sc1\ns_branch .Lpq\n.Lpq_ok:\nv_cmp_eq_u64 vcc, s[40:41], v[76:77]\nv_mov_b32 v145, v148\n"
"v_mov_b32 v147, v150\nds_write_b128 v13, v[144:147] offset:20480\nv_mov_b32 v153, v156\nv_mov_b32 v155, v158\n"
"ds_write_b128 v13, v[152:155] offset:21504\nv_mov_b32 v16, vcc_lo\nds_write_b64 v38, v[16:17] offset:18432\ns_waitcnt lgkmcnt(0)\ns_barrier\n"
"ds_write_b128 v8, v[48:51]\ns_branch .Ljoin\n.Lmf:\ns_getpc_b64 s[48:49]\n.Lpc:\ns_lshl_b32 s47, s2, 8\ns_and_b32 s47, s47, 0xf800\n"
"v_lshl_add_u32 v3, v2, 5, s47\nv_min_u32 v3, .Lend-.Lpc-4, v3\ns_cmp_lg_u32 s42, 0\ns_cbranch_scc1 .Lnopf\nglobal_load_dword v14, v3, s[48:49]\n"
".Lnopf:\ns_lshr_b32 s54, s42, 1\ns_and_b32 s55, s42, 1\ns_lshl_b32 s47, s54, 5\nv_and_b32 v3, 31, v2\ns_lshl_b32 s48, s54, 13\n"
"v_lshrrev_b32 v16, 5, v2\nv_bfe_u32 v17, v3, 1, 3\nv_xor_b32 v17, v16, v17\nv_lshlrev_b32 v17, 4, v17\nv_lshl_add_u32 v4, v3, 7, v17\n"
"v_add_u32 v4, s48, v4\nv_add_u32 v3, s47, v3\nv_xor_b32 v5, 32, v4\nv_xor_b32 v6, 64, v4\nv_xor_b32 v7, 96, v4\nv_bfe_u32 v17, v2, 3, 1\n"
"v_xor_b32 v17, s55, v17\nv_lshlrev_b32 v17, 6, v17\nv_bfe_u32 v18, v2, 4, 1\nv_lshl_add_u32 v17, v18, 5, v17\nv_and_b32 v18, 3, v2\n"
"v_lshl_add_u32 v17, v18, 3, v17\nv_bfe_u32 v18, v2, 2, 2\nv_lshl_add_u32 v17, v18, 7, v17\nv_lshl_add_u32 v8, v16, 10, v17\nv_add_u32 v8, s48, v8\n"
"s_lshl_b32 s48, s42, 12\nv_add_u32 v15, s48, v87\nv_xor_b32 v108, 0x2000, v15\nv_add_u32 v9, 0x10000, v8\nv_add_u32 v10, 0x20000, v8\n"
"v_bfe_u32 v80, v2, 2, 2\nv_bfe_u32 v17, v2, 4, 1\nv_cmp_eq_u32 vcc, v80, v17\nv_mov_b32 v17, 0x3f803f80\ns_nop 0\nv_cndmask_b32 v80, 0, v17, vcc\n"
"v_mov_b32 v81, v80\nv_mov_b32 v82, v80\nv_mov_b32 v83, v80\nv_lshlrev_b32 v12, 4, v3\nv_xor_b32 v109, 0x200, v12\nv_lshlrev_b32 v13, 4, v16\n"
"s_lshl_b32 s47, s54, 7\nv_add_u32 v13, s47, v13\nv_bfe_u32 v17, v2, 4, 1\nv_lshlrev_b32 v17, 16, v17\nv_lshl_add_u32 v17, v16, 8, v17\n"
"v_and_b32 v18, 15, v2\nv_lshl_add_u32 v11, v18, 4, v17\ns_lshl1_add_u32 s47, s46, s55\ns_lshl_b32 s47, s47, 17\ns_lshl1_add_u32 s62, s45, s54\n"
"s_lshl_b32 s62, s62, 10\ns_add_u32 s62, s62, s47\ns_lshl_b32 s63, s62, 1\ns_and_b32 s62, s62, 0xfffbffff\ns_lshl_b32 s47, s45, 16\n"
"s_and_b32 s47, s47, 0x40000\ns_or_b32 s62, s62, s47\ns_waitcnt lgkmcnt(0)\ns_add_u32 s40, s12, 0xa60000\ns_addc_u32 s41, s13, 0\n"
"s_load_dwordx2 s[40:41], s[40:41], 0x0\ns_mul_i32 s47, s43, 57344\ns_lshl_b32 s48, s43, 14\ns_mov_b64 exec, 1\n"
"buffer_load_dword v14, off, s[24:27], s47\nbuffer_load_dword v14, off, s[28:31], s47\nbuffer_load_dword v14, off, s[20:23], s48\ns_mov_b64 exec, -1\n"
"s_and_b32 s50, s46, 6\ns_bfe_u32 s47, s45, 0x10002\ns_or_b32 s50, s50, s47\ns_lshl_b32 s50, s50, 5\ns_add_u32 s50, s45, s50\ns_lshl_b32 s50, s50, 7\n"
"s_and_b32 s47, s46, 1\ns_mul_i32 s47, s47, 0x70000\ns_add_u32 s50, s50, s47\ns_add_u32 s50, s50, 0xa00000\ns_lshl_b32 s51, s44, 5\n"
"s_add_u32 s51, s43, s51\ns_lshl_b32 s51, s51, 7\ns_add_u32 s51, s51, 0xa10000\ns_lshl_b32 s48, s42, 10\ns_lshl_b32 s49, s43, 16\n"
"s_add_u32 s49, s49, s48\ns_add_u32 s49, s49, 0x200000\ns_and_b32 s56, s42, 3\ns_lshr_b32 s57, s42, 2\ns_mul_i32 s58, s44, 7\ns_add_u32 s58, s58, s42\n"
"s_lshl_b32 s21, s58, 6\ns_lshl_b32 s58, s58, 15\ns_add_u32 s58, s58, 0x1200000\ns_lshl3_add_u32 s59, s44, s42\ns_mul_i32 s59, s59, 28672\n"
"s_add_u32 s59, s59, 0x1400000\ns_lshl1_add_u32 s60, s56, s57\ns_lshl3_add_u32 s60, s44, s60\ns_lshl_b32 s60, s60, 13\ns_add_u32 s60, s60, 0x1600000\n"
"s_and_b32 s61, s56, 1\ns_lshl_b32 s61, s61, 14\ns_lshr_b32 s47, s56, 1\ns_lshl_b32 s47, s47, 16\ns_add_u32 s61, s61, s47\ns_mul_i32 s48, s43, 57344\n"
"s_add_u32 s21, s21, s48\ns_lshl_b32 s47, s43, 10\ns_add_u32 s54, s47, s58\ns_and_b32 s47, s43, 3\ns_mul_i32 s47, s47, 7\ns_add_u32 s47, s47, s42\n"
"s_lshr_b32 s48, s43, 2\ns_mul_i32 s22, s48, 14\ns_lshr_b32 s0, s47, 1\ns_add_u32 s22, s22, s0\ns_lshl_b32 s22, s22, 14\ns_lshr_b32 s0, s48, 1\n"
"s_xor_b32 s0, s0, s47\ns_and_b32 s0, s0, 1\ns_lshl_b32 s0, s0, 6\ns_add_u32 s22, s22, s0\ns_lshl_b32 s0, s44, 7\ns_add_u32 s22, s22, s0\n"
"s_and_b32 s52, s48, 1\ns_lshr_b32 s48, s48, 2\ns_lshl1_add_u32 s52, s52, s48\ns_lshl_b32 s52, s52, 4\ns_mul_i32 s47, s44, 224\ns_mul_i32 s48, s43, 7\n"
"s_add_u32 s47, s47, s48\ns_add_u32 s47, s47, s42\ns_lshl_b32 s47, s47, 10\ns_add_u32 s55, s47, 0x1400000\ns_lshl_b32 s47, s43, 14\n"
"s_add_u32 s0, s0, s47\ns_lshr_b32 s48, s43, 4\ns_lshl_b32 s48, s48, 6\ns_add_u32 s0, s0, s48\ns_xor_b32 s101, s0, 64\ns_lshl_b32 s3, s43, 2\n"
"s_and_b32 s3, s3, 32\ns_lshl_b32 s47, s44, 16\ns_lshl_b32 s48, s43, 11\ns_add_u32 s47, s47, s48\ns_add_u32 s47, s47, 0x1600000\n"
"s_add_u32 s48, s47, 0x400\ns_movk_i32 s23, 0xe00\ns_cmp_eq_u32 s42, 7\ns_cselect_b32 s54, s47, s54\ns_cselect_b32 s55, s48, s55\n"
"s_cselect_b32 s21, s0, s21\ns_cselect_b32 s22, s101, s22\ns_cselect_b32 s23, 0x400, s23\ns_cselect_b32 s52, s3, s52\ns_cselect_b32 s3, s3, 0\n"
"s_lshl_b32 s20, s42, 11\ns_add_u32 s20, s20, 0x21000\ns_waitcnt lgkmcnt(0)\ns_bitcmp1_b32 s46, 0\ns_cbranch_scc1 .Lmv\nv_lshrrev_b32 v17, 1, v87\n"
"v_xor_b32 v17, v17, v87\nv_and_b32 v17, 64, v17\nv_mul_u32_u24 v17, 3, v17\nv_xor_b32 v11, v17, v87\ns_barrier\ns_add_u32 s40, s40, 0x9e3779b9\n"
"s_xor_b32 s41, s41, 0x85ebca6b\nds_read_b64_tr_b16 v[112:113], v9 offset:0\nds_read_b64_tr_b16 v[114:115], v9 offset:512\n"
"ds_read_b128 v[132:135], v4 offset:4096\nds_read_b128 v[128:131], v4 offset:0\nds_read_b64_tr_b16 v[116:117], v9 offset:4096\n"
"ds_read_b64_tr_b16 v[118:119], v9 offset:4608\nds_read_b128 v[140:143], v6 offset:4096\nds_read_b128 v[136:139], v6 offset:0\n"
"ds_read_b128 v[148:151], v5 offset:4096\nds_read_b128 v[144:147], v5 offset:0\nds_read_b128 v[156:159], v7 offset:4096\n"
"ds_read_b128 v[152:155], v7 offset:0\nds_read_b64_tr_b16 v[120:121], v9 offset:2048\nds_read_b64_tr_b16 v[122:123], v9 offset:2560\n"
"ds_read_b64_tr_b16 v[124:125], v9 offset:6144\nds_read_b64_tr_b16 v[126:127], v9 offset:6656\ns_waitcnt lgkmcnt(14)\n"
"v_mfma_f32_16x16x32_bf16 v[64:67], v[80:83], v[112:115], 0\ns_waitcnt lgkmcnt(13)\nv_mfma_f32_32x32x16_bf16 v[32:47], v[112:115], v[132:135], 0\n"
"s_waitcnt lgkmcnt(12)\nv_mfma_f32_32x32x16_bf16 v[16:31], v[112:115], v[128:131], 0\ns_waitcnt lgkmcnt(10)\n"
"v_mfma_f32_16x16x32_bf16 v[64:67], v[80:83], v[116:119], v[64:67]\ns_waitcnt lgkmcnt(9)\n"
"v_mfma_f32_32x32x16_bf16 v[32:47], v[116:119], v[140:143], v[32:47]\ns_waitcnt lgkmcnt(8)\n"
"v_mfma_f32_32x32x16_bf16 v[16:31], v[116:119], v[136:139], v[16:31]\ns_waitcnt lgkmcnt(4)\ns_waitcnt lgkmcnt(0)\ns_barrier\n"
"ds_read_b64_tr_b16 v[112:113], v9 offset:16384\nds_read_b64_tr_b16 v[114:115], v9 offset:16896\nds_read_b64_tr_b16 v[116:117], v9 offset:20480\n"
"ds_read_b64_tr_b16 v[118:119], v9 offset:20992\nv_mfma_f32_16x16x32_bf16 v[64:67], v[80:83], v[120:123], v[64:67]\n"
"v_mfma_f32_32x32x16_bf16 v[32:47], v[120:123], v[148:151], v[32:47]\nds_read_b128 v[164:167], v4 offset:20480\n"
"ds_read_b128 v[160:163], v4 offset:16384\nv_mfma_f32_32x32x16_bf16 v[16:31], v[120:123], v[144:147], v[16:31]\n"
"ds_read_b128 v[172:175], v6 offset:20480\nds_read_b128 v[168:171], v6 offset:16384\nds_read_b64_tr_b16 v[120:121], v9 offset:18432\n"
"ds_read_b64_tr_b16 v[122:123], v9 offset:18944\nv_mfma_f32_16x16x32_bf16 v[64:67], v[80:83], v[124:127], v[64:67]\n"
"v_mfma_f32_32x32x16_bf16 v[32:47], v[124:127], v[156:159], v[32:47]\nds_read_b128 v[180:183], v5 offset:20480\n"
"ds_read_b128 v[176:179], v5 offset:16384\nv_mfma_f32_32x32x16_bf16 v[16:31], v[124:127], v[152:155], v[16:31]\n"
"ds_read_b128 v[188:191], v7 offset:20480\nds_read_b128 v[184:187], v7 offset:16384\nds_read_b64_tr_b16 v[124:125], v9 offset:22528\n"
"ds_read_b64_tr_b16 v[126:127], v9 offset:23040\ns_waitcnt lgkmcnt(14)\nv_mfma_f32_16x16x32_bf16 v[64:67], v[80:83], v[112:115], v[64:67]\n"
"s_waitcnt lgkmcnt(11)\nv_mfma_f32_32x32x16_bf16 v[32:47], v[112:115], v[164:167], v[32:47]\nds_read_b128 v[196:199], v4 offset:36864\n"
"ds_read_b128 v[192:195], v4 offset:32768\ns_waitcnt lgkmcnt(12)\nv_mfma_f32_32x32x16_bf16 v[16:31], v[112:115], v[160:163], v[16:31]\n"
"ds_read_b128 v[204:207], v6 offset:36864\nds_read_b128 v[200:203], v6 offset:32768\n"
"v_mfma_f32_16x16x32_bf16 v[64:67], v[80:83], v[116:119], v[64:67]\ns_waitcnt lgkmcnt(13)\n"
"v_mfma_f32_32x32x16_bf16 v[32:47], v[116:119], v[172:175], v[32:47]\nds_read_b128 v[212:215], v5 offset:36864\n"
"ds_read_b128 v[208:211], v5 offset:32768\ns_waitcnt lgkmcnt(14)\nv_mfma_f32_32x32x16_bf16 v[16:31], v[116:119], v[168:171], v[16:31]\n"
"ds_read_b128 v[220:223], v7 offset:36864\nds_read_b128 v[216:219], v7 offset:32768\ns_waitcnt lgkmcnt(10)\ns_waitcnt lgkmcnt(8)\ns_barrier\n"
"ds_read_b64_tr_b16 v[112:113], v9 offset:32768\nds_read_b64_tr_b16 v[114:115], v9 offset:33280\nds_read_b64_tr_b16 v[116:117], v9 offset:36864\n"
"ds_read_b64_tr_b16 v[118:119], v9 offset:37376\nv_mfma_f32_16x16x32_bf16 v[64:67], v[80:83], v[120:123], v[64:67]\n"
"v_mfma_f32_32x32x16_bf16 v[32:47], v[120:123], v[180:183], v[32:47]\nds_read_b128 v[228:231], v4 offset:53248\n"
"ds_read_b128 v[224:227], v4 offset:49152\nv_mfma_f32_32x32x16_bf16 v[16:31], v[120:123], v[176:179], v[16:31]\n"
"ds_read_b128 v[236:239], v6 offset:53248\nds_read_b128 v[232:235], v6 offset:49152\nds_read_b64_tr_b16 v[120:121], v9 offset:34816\n"
"ds_read_b64_tr_b16 v[122:123], v9 offset:35328\nv_mfma_f32_16x16x32_bf16 v[64:67], v[80:83], v[124:127], v[64:67]\n"
"v_mfma_f32_32x32x16_bf16 v[32:47], v[124:127], v[188:191], v[32:47]\nds_read_b128 v[244:247], v5 offset:53248\n"
"ds_read_b128 v[240:243], v5 offset:49152\nv_mfma_f32_32x32x16_bf16 v[16:31], v[124:127], v[184:187], v[16:31]\n"
"ds_read_b128 v[252:255], v7 offset:53248\nds_read_b128 v[248:251], v7 offset:49152\nds_read_b64_tr_b16 v[124:125], v9 offset:38912\n"
"ds_read_b64_tr_b16 v[126:127], v9 offset:39424\ns_waitcnt lgkmcnt(14)\nv_mfma_f32_16x16x32_bf16 v[64:67], v[80:83], v[112:115], v[64:67]\n"
"v_mfma_f32_32x32x16_bf16 v[32:47], v[112:115], v[196:199], v[32:47]\nv_mfma_f32_32x32x16_bf16 v[16:31], v[112:115], v[192:195], v[16:31]\n"
"s_waitcnt lgkmcnt(12)\nv_mfma_f32_16x16x32_bf16 v[64:67], v[80:83], v[116:119], v[64:67]\n"
"v_mfma_f32_32x32x16_bf16 v[32:47], v[116:119], v[204:207], v[32:47]\nv_mfma_f32_32x32x16_bf16 v[16:31], v[116:119], v[200:203], v[16:31]\n"
"s_waitcnt lgkmcnt(2)\ns_waitcnt lgkmcnt(0)\ns_barrier\nds_read_b64_tr_b16 v[112:113], v9 offset:49152\n"
"ds_read_b64_tr_b16 v[114:115], v9 offset:49664\nds_read_b64_tr_b16 v[116:117], v9 offset:53248\nds_read_b64_tr_b16 v[118:119], v9 offset:53760\n"
"v_mfma_f32_16x16x32_bf16 v[64:67], v[80:83], v[120:123], v[64:67]\nv_mfma_f32_32x32x16_bf16 v[32:47], v[120:123], v[212:215], v[32:47]\n"
"v_mfma_f32_32x32x16_bf16 v[16:31], v[120:123], v[208:211], v[16:31]\nds_read_b64_tr_b16 v[120:121], v9 offset:51200\n"
"ds_read_b64_tr_b16 v[122:123], v9 offset:51712\nv_mfma_f32_16x16x32_bf16 v[64:67], v[80:83], v[124:127], v[64:67]\n"
"v_mfma_f32_32x32x16_bf16 v[32:47], v[124:127], v[220:223], v[32:47]\nv_mfma_f32_32x32x16_bf16 v[16:31], v[124:127], v[216:219], v[16:31]\n"
"ds_read_b64_tr_b16 v[124:125], v9 offset:55296\nds_read_b64_tr_b16 v[126:127], v9 offset:55808\ns_waitcnt lgkmcnt(6)\n"
"v_mfma_f32_16x16x32_bf16 v[64:67], v[80:83], v[112:115], v[64:67]\nv_mfma_f32_32x32x16_bf16 v[32:47], v[112:115], v[228:231], v[32:47]\n"
"v_mfma_f32_32x32x16_bf16 v[16:31], v[112:115], v[224:227], v[16:31]\ns_waitcnt lgkmcnt(4)\n"
"v_mfma_f32_16x16x32_bf16 v[64:67], v[80:83], v[116:119], v[64:67]\nv_mfma_f32_32x32x16_bf16 v[32:47], v[116:119], v[236:239], v[32:47]\n"
"v_mfma_f32_32x32x16_bf16 v[16:31], v[116:119], v[232:235], v[16:31]\ns_waitcnt lgkmcnt(0)\ns_barrier\nds_read_b64_tr_b16 v[112:113], v10 offset:0\n"
"ds_read_b64_tr_b16 v[114:115], v10 offset:512\nds_read_b64_tr_b16 v[116:117], v10 offset:4096\nds_read_b64_tr_b16 v[118:119], v10 offset:4608\n"
"ds_read_b128 v[104:107], v12 offset:16384\nds_read_b64 v[110:111], v109 offset:16384\n"
"v_mfma_f32_16x16x32_bf16 v[64:67], v[80:83], v[120:123], v[64:67]\nv_mfma_f32_32x32x16_bf16 v[32:47], v[120:123], v[244:247], v[32:47]\n"
"v_mfma_f32_32x32x16_bf16 v[16:31], v[120:123], v[240:243], v[16:31]\nds_read_b64_tr_b16 v[120:121], v10 offset:2048\n"
"ds_read_b64_tr_b16 v[122:123], v10 offset:2560\nv_mfma_f32_16x16x32_bf16 v[64:67], v[80:83], v[124:127], v[64:67]\n"
"v_mfma_f32_32x32x16_bf16 v[32:47], v[124:127], v[252:255], v[32:47]\nv_mfma_f32_32x32x16_bf16 v[16:31], v[124:127], v[248:251], v[16:31]\n"
"ds_read_b64_tr_b16 v[124:125], v10 offset:6144\nds_read_b64_tr_b16 v[126:127], v10 offset:6656\ns_waitcnt lgkmcnt(8)\n"
"v_mfma_f32_16x16x32_bf16 v[68:71], v[80:83], v[112:115], 0\nv_mfma_f32_32x32x16_bf16 v[88:103], v[112:115], v[132:135], 0\n"
"v_cvt_pk_bf16_f32 v2, v64, v64\nv_and_b32 v2, 0xffff0000, v2\nv_sub_f32 v3, v64, v2\nv_cvt_pk_bf16_f32 v0, v64, v3\nv_and_b32 v2, 0xffff0000, v0\n"
"v_sub_f32 v3, v3, v2\nv_cvt_pk_bf16_f32 v1, v3, 0\ns_waitcnt lgkmcnt(4)\ns_nop 0\nv_mfma_f32_32x32x8_bf16 v[32:47], v[0:1], v[110:111], v[32:47]\n"
"v_mfma_f32_32x32x16_bf16 v[48:63], v[112:115], v[128:131], 0\nv_mfma_f32_32x32x8_bf16 v[16:31], v[0:1], v[104:105], v[16:31]\n"
"v_mfma_f32_16x16x32_bf16 v[68:71], v[80:83], v[116:119], v[68:71]\nds_write_b128 v15, v[32:35] offset:32768\n"
"v_mfma_f32_32x32x16_bf16 v[88:103], v[116:119], v[140:143], v[88:103]\nds_write_b128 v15, v[36:39] offset:33792\n"
"ds_write_b128 v15, v[40:43] offset:34816\nv_mfma_f32_32x32x16_bf16 v[48:63], v[116:119], v[136:139], v[48:63]\n"
"ds_write_b128 v15, v[44:47] offset:35840\ns_waitcnt lgkmcnt(4)\ns_waitcnt lgkmcnt(0)\ns_barrier\nds_read_b64_tr_b16 v[112:113], v10 offset:16384\n"
"ds_read_b64_tr_b16 v[114:115], v10 offset:16896\nds_read_b64_tr_b16 v[116:117], v10 offset:20480\nds_read_b64_tr_b16 v[118:119], v10 offset:20992\n"
"ds_read_b128 v[32:35], v108 offset:32768\nds_read_b128 v[36:39], v108 offset:33792\nds_read_b128 v[40:43], v108 offset:34816\n"
"ds_read_b128 v[44:47], v108 offset:35840\nv_mfma_f32_16x16x32_bf16 v[68:71], v[80:83], v[120:123], v[68:71]\n"
"v_mfma_f32_32x32x16_bf16 v[88:103], v[120:123], v[148:151], v[88:103]\nv_mfma_f32_32x32x16_bf16 v[48:63], v[120:123], v[144:147], v[48:63]\n"
"ds_read_b64_tr_b16 v[120:121], v10 offset:18432\nds_read_b64_tr_b16 v[122:123], v10 offset:18944\n"
"v_mfma_f32_16x16x32_bf16 v[68:71], v[80:83], v[124:127], v[68:71]\ns_waitcnt lgkmcnt(5)\nv_pk_add_f32 v[16:17], v[16:17], v[32:33]\n"
"v_pk_add_f32 v[18:19], v[18:19], v[34:35]\ns_waitcnt lgkmcnt(4)\nv_pk_add_f32 v[20:21], v[20:21], v[36:37]\n"
"v_pk_add_f32 v[22:23], v[22:23], v[38:39]\nv_mfma_f32_32x32x16_bf16 v[88:103], v[124:127], v[156:159], v[88:103]\n"
"v_pk_mul_f32 v[16:17], v[16:17], v[106:107] op_sel:[0,0] op_sel_hi:[1,0]\nv_pk_mul_f32 v[18:19], v[18:19], v[106:107] op_sel:[0,0] op_sel_hi:[1,0]\n"
"v_pk_mul_f32 v[20:21], v[20:21], v[106:107] op_sel:[0,0] op_sel_hi:[1,0]\nv_pk_mul_f32 v[22:23], v[22:23], v[106:107] op_sel:[0,0] op_sel_hi:[1,0]\n"
"v_mfma_f32_32x32x16_bf16 v[48:63], v[124:127], v[152:155], v[48:63]\nv_cvt_pk_bf16_f32 v16, v16, v17\nv_cvt_pk_bf16_f32 v17, v18, v19\n"
"v_cvt_pk_bf16_f32 v18, v20, v21\nv_cvt_pk_bf16_f32 v19, v22, v23\ns_add_u32 s53, s62, 0x200000\n"
"buffer_store_dwordx4 v[16:19], v11, s[12:15], s53 offen sc1\nds_read_b64_tr_b16 v[124:125], v10 offset:22528\n"
"ds_read_b64_tr_b16 v[126:127], v10 offset:23040\nv_mfma_f32_16x16x32_bf16 v[68:71], v[80:83], v[112:115], v[68:71]\ns_waitcnt lgkmcnt(5)\n"
"v_pk_add_f32 v[24:25], v[24:25], v[40:41]\nv_pk_add_f32 v[26:27], v[26:27], v[42:43]\ns_waitcnt lgkmcnt(4)\n"
"v_pk_add_f32 v[28:29], v[28:29], v[44:45]\nv_pk_add_f32 v[30:31], v[30:31], v[46:47]\n"
"v_mfma_f32_32x32x16_bf16 v[88:103], v[112:115], v[164:167], v[88:103]\nv_pk_mul_f32 v[24:25], v[24:25], v[106:107] op_sel:[0,0] op_sel_hi:[1,0]\n"
"v_pk_mul_f32 v[26:27], v[26:27], v[106:107] op_sel:[0,0] op_sel_hi:[1,0]\nv_pk_mul_f32 v[28:29], v[28:29], v[106:107] op_sel:[0,0] op_sel_hi:[1,0]\n"
"v_pk_mul_f32 v[30:31], v[30:31], v[106:107] op_sel:[0,0] op_sel_hi:[1,0]\nv_mfma_f32_32x32x16_bf16 v[48:63], v[112:115], v[160:163], v[48:63]\n"
"v_cvt_pk_bf16_f32 v24, v24, v25\nv_cvt_pk_bf16_f32 v25, v26, v27\nv_cvt_pk_bf16_f32 v26, v28, v29\nv_cvt_pk_bf16_f32 v27, v30, v31\n"
"s_add_u32 s53, s62, 0x210000\nbuffer_store_dwordx4 v[24:27], v11, s[12:15], s53 offen sc1\n"
"v_mfma_f32_16x16x32_bf16 v[68:71], v[80:83], v[116:119], v[68:71]\nv_mfma_f32_32x32x16_bf16 v[88:103], v[116:119], v[172:175], v[88:103]\n"
"v_mov_b32 v73, s40\nv_mov_b32 v75, s40\nv_mov_b32 v77, s40\nv_mov_b32 v79, s40\nv_mfma_f32_32x32x16_bf16 v[48:63], v[116:119], v[168:171], v[48:63]\n"
"s_waitcnt lgkmcnt(0)\ns_barrier\nds_read_b64_tr_b16 v[112:113], v9 offset:0\nds_read_b64_tr_b16 v[114:115], v9 offset:512\n"
"ds_read_b64_tr_b16 v[116:117], v9 offset:4096\nds_read_b64_tr_b16 v[118:119], v9 offset:4608\n"
"v_mfma_f32_16x16x32_bf16 v[68:71], v[80:83], v[120:123], v[68:71]\nv_mfma_f32_32x32x16_bf16 v[88:103], v[120:123], v[180:183], v[88:103]\n"
"v_mfma_f32_32x32x16_bf16 v[48:63], v[120:123], v[176:179], v[48:63]\nds_read_b64_tr_b16 v[120:121], v9 offset:2048\n"
"ds_read_b64_tr_b16 v[122:123], v9 offset:2560\nv_mfma_f32_16x16x32_bf16 v[68:71], v[80:83], v[124:127], v[68:71]\n"
"v_mfma_f32_32x32x16_bf16 v[88:103], v[124:127], v[188:191], v[88:103]\nv_mfma_f32_32x32x16_bf16 v[48:63], v[124:127], v[184:187], v[48:63]\n"
"ds_read_b64_tr_b16 v[124:125], v9 offset:6144\nds_read_b64_tr_b16 v[126:127], v9 offset:6656\ns_waitcnt lgkmcnt(6)\n"
"v_mfma_f32_16x16x32_bf16 v[68:71], v[80:83], v[112:115], v[68:71]\nv_mfma_f32_32x32x16_bf16 v[88:103], v[112:115], v[196:199], v[88:103]\n"
"v_mfma_f32_32x32x16_bf16 v[48:63], v[112:115], v[192:195], v[48:63]\ns_waitcnt lgkmcnt(4)\n"
"v_mfma_f32_16x16x32_bf16 v[68:71], v[80:83], v[116:119], v[68:71]\nv_mfma_f32_32x32x16_bf16 v[88:103], v[116:119], v[204:207], v[88:103]\n"
"v_mfma_f32_32x32x16_bf16 v[48:63], v[116:119], v[200:203], v[48:63]\ns_waitcnt lgkmcnt(0)\ns_waitcnt vmcnt(0)\ns_barrier\n"
"ds_read_b64_tr_b16 v[112:113], v8 offset:0\nds_read_b64_tr_b16 v[114:115], v8 offset:512\nds_read_b64_tr_b16 v[116:117], v8 offset:4096\n"
"ds_read_b64_tr_b16 v[118:119], v8 offset:4608\nv_mfma_f32_16x16x32_bf16 v[68:71], v[80:83], v[120:123], v[68:71]\n"
"v_mfma_f32_32x32x16_bf16 v[88:103], v[120:123], v[212:215], v[88:103]\nv_mfma_f32_32x32x16_bf16 v[48:63], v[120:123], v[208:211], v[48:63]\n"
"ds_read_b64_tr_b16 v[120:121], v8 offset:2048\nds_read_b64_tr_b16 v[122:123], v8 offset:2560\n"
"v_mfma_f32_16x16x32_bf16 v[68:71], v[80:83], v[124:127], v[68:71]\nv_mfma_f32_32x32x16_bf16 v[88:103], v[124:127], v[220:223], v[88:103]\n"
"v_mfma_f32_32x32x16_bf16 v[48:63], v[124:127], v[216:219], v[48:63]\nds_read_b64_tr_b16 v[124:125], v8 offset:6144\n"
"ds_read_b64_tr_b16 v[126:127], v8 offset:6656\ns_waitcnt lgkmcnt(6)\nv_mfma_f32_16x16x32_bf16 v[68:71], v[80:83], v[112:115], v[68:71]\n"
"v_mfma_f32_32x32x16_bf16 v[88:103], v[112:115], v[228:231], v[88:103]\nv_mfma_f32_32x32x16_bf16 v[48:63], v[112:115], v[224:227], v[48:63]\n"
"s_waitcnt lgkmcnt(4)\nv_mfma_f32_16x16x32_bf16 v[68:71], v[80:83], v[116:119], v[68:71]\n"
"v_mfma_f32_32x32x16_bf16 v[88:103], v[116:119], v[236:239], v[88:103]\nv_mfma_f32_32x32x16_bf16 v[48:63], v[116:119], v[232:235], v[48:63]\n"
"s_waitcnt lgkmcnt(2)\nv_mfma_f32_16x16x32_bf16 v[68:71], v[80:83], v[120:123], v[68:71]\ns_waitcnt lgkmcnt(0)\n"
"v_mfma_f32_16x16x32_bf16 v[68:71], v[80:83], v[124:127], v[68:71]\nv_mfma_f32_32x32x16_bf16 v[88:103], v[120:123], v[244:247], v[88:103]\ns_nop 4\n"
"v_cvt_pk_bf16_f32 v2, v68, v68\nv_and_b32 v2, 0xffff0000, v2\nv_sub_f32 v3, v68, v2\nv_cvt_pk_bf16_f32 v0, v68, v3\nv_and_b32 v2, 0xffff0000, v0\n"
"v_sub_f32 v3, v3, v2\nv_cvt_pk_bf16_f32 v1, v3, 0\nv_mfma_f32_32x32x16_bf16 v[88:103], v[124:127], v[252:255], v[88:103]\ns_nop 0\n"
"v_mfma_f32_32x32x8_bf16 v[88:103], v[0:1], v[110:111], v[88:103]\nv_mfma_f32_32x32x16_bf16 v[48:63], v[120:123], v[240:243], v[48:63]\n"
"v_mfma_f32_32x32x16_bf16 v[48:63], v[124:127], v[248:251], v[48:63]\ns_nop 1\nds_write_b128 v15, v[88:91] offset:32768\n"
"ds_write_b128 v15, v[92:95] offset:33792\nv_mfma_f32_32x32x8_bf16 v[48:63], v[0:1], v[104:105], v[48:63]\nds_write_b128 v15, v[96:99] offset:34816\n"
"ds_write_b128 v15, v[100:103] offset:35840\ns_waitcnt lgkmcnt(0)\ns_barrier\nds_read_b128 v[88:91], v108 offset:32768\n"
"ds_read_b128 v[92:95], v108 offset:33792\nds_read_b128 v[96:99], v108 offset:34816\nds_read_b128 v[100:103], v108 offset:35840\n"
"ds_read_b64_tr_b16 v[112:113], v9 offset:16384\nds_read_b64_tr_b16 v[114:115], v9 offset:16896\nds_read_b64_tr_b16 v[116:117], v9 offset:20480\n"
"ds_read_b64_tr_b16 v[118:119], v9 offset:20992\nds_read_b64_tr_b16 v[120:121], v9 offset:18432\nds_read_b64_tr_b16 v[122:123], v9 offset:18944\n"
"ds_read_b64_tr_b16 v[124:125], v9 offset:22528\nds_read_b64_tr_b16 v[126:127], v9 offset:23040\ns_waitcnt lgkmcnt(11)\n"
"v_pk_add_f32 v[48:49], v[48:49], v[88:89]\nv_pk_add_f32 v[50:51], v[50:51], v[90:91]\ns_waitcnt lgkmcnt(10)\n"
"v_pk_add_f32 v[52:53], v[52:53], v[92:93]\nv_pk_add_f32 v[54:55], v[54:55], v[94:95]\ns_waitcnt lgkmcnt(6)\n"
"v_mfma_f32_16x16x32_bf16 v[64:67], v[80:83], v[112:115], 0\nv_pk_mul_f32 v[48:49], v[48:49], v[106:107] op_sel:[0,1] op_sel_hi:[1,1]\n"
"v_pk_mul_f32 v[50:51], v[50:51], v[106:107] op_sel:[0,1] op_sel_hi:[1,1]\nv_pk_mul_f32 v[52:53], v[52:53], v[106:107] op_sel:[0,1] op_sel_hi:[1,1]\n"
"v_pk_mul_f32 v[54:55], v[54:55], v[106:107] op_sel:[0,1] op_sel_hi:[1,1]\nv_mfma_f32_32x32x16_bf16 v[32:47], v[112:115], v[132:135], 0\n"
"s_add_u32 s53, s63, 0x1800000\nv_cvt_pk_bf16_f32 v72, v48, v49\nv_cvt_pk_bf16_f32 v74, v52, v53\n"
"buffer_store_dwordx4 v[72:75], v87, s[12:15], s53 offen sc1\nv_cvt_pk_bf16_f32 v76, v50, v51\nv_cvt_pk_bf16_f32 v78, v54, v55\n"
"buffer_store_dwordx4 v[76:79], v87, s[12:15], s53 offen offset:1024 sc1\nv_mfma_f32_32x32x16_bf16 v[16:31], v[112:115], v[128:131], 0\n"
"v_pk_add_f32 v[56:57], v[56:57], v[96:97]\nv_pk_add_f32 v[58:59], v[58:59], v[98:99]\nv_pk_add_f32 v[60:61], v[60:61], v[100:101]\n"
"v_pk_add_f32 v[62:63], v[62:63], v[102:103]\nv_pk_mul_f32 v[56:57], v[56:57], v[106:107] op_sel:[0,1] op_sel_hi:[1,1]\n"
"v_pk_mul_f32 v[58:59], v[58:59], v[106:107] op_sel:[0,1] op_sel_hi:[1,1]\nv_pk_mul_f32 v[60:61], v[60:61], v[106:107] op_sel:[0,1] op_sel_hi:[1,1]\n"
"v_pk_mul_f32 v[62:63], v[62:63], v[106:107] op_sel:[0,1] op_sel_hi:[1,1]\ns_waitcnt lgkmcnt(4)\n"
"v_mfma_f32_16x16x32_bf16 v[64:67], v[80:83], v[116:119], v[64:67]\ns_add_u32 s53, s63, 0x1820000\nv_cvt_pk_bf16_f32 v72, v56, v57\n"
"v_cvt_pk_bf16_f32 v74, v60, v61\nbuffer_store_dwordx4 v[72:75], v87, s[12:15], s53 offen sc1\nv_cvt_pk_bf16_f32 v76, v58, v59\n"
"v_cvt_pk_bf16_f32 v78, v62, v63\nbuffer_store_dwordx4 v[76:79], v87, s[12:15], s53 offen offset:1024 sc1\n"
"v_mfma_f32_32x32x16_bf16 v[32:47], v[116:119], v[140:143], v[32:47]\nv_mfma_f32_32x32x16_bf16 v[16:31], v[116:119], v[136:139], v[16:31]\ns_barrier\n"
"ds_read_b64_tr_b16 v[112:113], v9 offset:32768\nds_read_b64_tr_b16 v[114:115], v9 offset:33280\nds_read_b64_tr_b16 v[116:117], v9 offset:36864\n"
"ds_read_b64_tr_b16 v[118:119], v9 offset:37376\ns_waitcnt lgkmcnt(6)\nv_mfma_f32_16x16x32_bf16 v[64:67], v[80:83], v[120:123], v[64:67]\n"
"v_mfma_f32_32x32x16_bf16 v[32:47], v[120:123], v[148:151], v[32:47]\nv_mfma_f32_32x32x16_bf16 v[16:31], v[120:123], v[144:147], v[16:31]\n"
"ds_read_b64_tr_b16 v[120:121], v9 offset:34816\nds_read_b64_tr_b16 v[122:123], v9 offset:35328\ns_waitcnt lgkmcnt(6)\n"
"v_mfma_f32_16x16x32_bf16 v[64:67], v[80:83], v[124:127], v[64:67]\nv_mfma_f32_32x32x16_bf16 v[32:47], v[124:127], v[156:159], v[32:47]\n"
"v_lshrrev_b32 v2, 4, v87\ns_xor_b32 s62, s62, 0x40000\nv_lshrrev_b32 v4, 2, v2\nv_mfma_f32_32x32x16_bf16 v[16:31], v[124:127], v[152:155], v[16:31]\n"
"v_and_b32 v5, 3, v2\nv_lshlrev_b32 v5, 4, v5\nv_xor_b32 v6, s3, v5\nds_read_b64_tr_b16 v[124:125], v9 offset:38912\n"
"ds_read_b64_tr_b16 v[126:127], v9 offset:39424\ns_waitcnt lgkmcnt(6)\nv_mfma_f32_16x16x32_bf16 v[64:67], v[80:83], v[112:115], v[64:67]\n"
"v_mfma_f32_32x32x16_bf16 v[32:47], v[112:115], v[164:167], v[32:47]\nv_mad_u32_u24 v100, v4, s23, v6\nv_xor_b32 v6, s52, v5\n"
"v_lshl_add_u32 v101, v4, 10, v6\nv_mfma_f32_32x32x16_bf16 v[16:31], v[112:115], v[160:163], v[16:31]\nv_lshrrev_b32 v4, 5, v2\n"
"v_bfe_u32 v5, v2, 2, 2\nv_lshl_add_u32 v6, v4, 2, v5\nds_read_b64_tr_b16 v[112:113], v9 offset:49152\nds_read_b64_tr_b16 v[114:115], v9 offset:49664\n"
"s_waitcnt lgkmcnt(6)\nv_mfma_f32_16x16x32_bf16 v[64:67], v[80:83], v[116:119], v[64:67]\n"
"v_mfma_f32_32x32x16_bf16 v[32:47], v[116:119], v[172:175], v[32:47]\nv_lshlrev_b32 v6, 6, v6\nv_bfe_u32 v5, v2, 4, 1\nv_lshl_add_u32 v6, v5, 5, v6\n"
"v_mfma_f32_32x32x16_bf16 v[16:31], v[116:119], v[168:171], v[16:31]\nv_and_b32 v3, 3, v2\nv_lshl_add_u32 v6, v3, 3, v6\nv_add_u32 v102, s20, v6\n"
"ds_read_b64_tr_b16 v[116:117], v9 offset:53248\nds_read_b64_tr_b16 v[118:119], v9 offset:53760\ns_waitcnt lgkmcnt(6)\n"
"v_mfma_f32_16x16x32_bf16 v[64:67], v[80:83], v[120:123], v[64:67]\nv_mfma_f32_32x32x16_bf16 v[32:47], v[120:123], v[180:183], v[32:47]\n"
"v_and_b32 v3, 15, v2\nv_lshlrev_b32 v3, 4, v3\nv_lshl_add_u32 v3, v4, 8, v3\nv_mfma_f32_32x32x16_bf16 v[16:31], v[120:123], v[176:179], v[16:31]\n"
"v_add_u32 v6, 1, v5\nv_lshl_add_u32 v72, v6, 16, v3\nv_xor_b32 v5, 1, v5\nds_read_b64_tr_b16 v[120:121], v9 offset:51200\n"
"ds_read_b64_tr_b16 v[122:123], v9 offset:51712\ns_waitcnt lgkmcnt(6)\nv_mfma_f32_16x16x32_bf16 v[64:67], v[80:83], v[124:127], v[64:67]\n"
"v_mfma_f32_32x32x16_bf16 v[32:47], v[124:127], v[188:191], v[32:47]\nv_lshlrev_b32 v73, 12, v5\ns_lshl_b32 s48, s42, 10\nv_add_u32 v74, s48, v87\n"
"v_mfma_f32_32x32x16_bf16 v[16:31], v[124:127], v[184:187], v[16:31]\nv_add_u32 v75, 0x10000, v74\nv_and_b32 v3, 7, v2\nv_bfe_u32 v4, v2, 3, 1\n"
"ds_read_b64_tr_b16 v[124:125], v9 offset:55296\nds_read_b64_tr_b16 v[126:127], v9 offset:55808\ns_waitcnt lgkmcnt(6)\n"
"v_mfma_f32_16x16x32_bf16 v[64:67], v[80:83], v[112:115], v[64:67]\nv_mfma_f32_32x32x16_bf16 v[32:47], v[112:115], v[196:199], v[32:47]\n"
"v_mul_u32_u24 v4, 0x70000, v4\nv_lshl_add_u32 v3, v3, 9, v4\ns_lshr_b32 s47, s42, 1\n"
"v_mfma_f32_32x32x16_bf16 v[16:31], v[112:115], v[192:195], v[16:31]\ns_lshl_b32 s47, s47, 7\ns_lshr_b32 s48, s43, 2\ns_lshl_b32 s48, s48, 12\n"
"ds_read_b64_tr_b16 v[112:113], v8 offset:49152\nds_read_b64_tr_b16 v[114:115], v8 offset:49664\ns_waitcnt lgkmcnt(6)\n"
"v_mfma_f32_16x16x32_bf16 v[64:67], v[80:83], v[116:119], v[64:67]\nv_mfma_f32_32x32x16_bf16 v[32:47], v[116:119], v[204:207], v[32:47]\n"
"s_add_u32 s47, s47, s48\ns_add_u32 s47, s47, 0xa00000\nv_add_u32 v78, s47, v3\nv_mfma_f32_32x32x16_bf16 v[16:31], v[116:119], v[200:203], v[16:31]\n"
"s_cmp_eq_u32 s42, 7\ns_cselect_b32 s47, 0x40000000, 0\nv_add_u32 v79, s47, v87\nv_and_b32 v3, 31, v2\nds_read_b64_tr_b16 v[116:117], v8 offset:53248\n"
"ds_read_b64_tr_b16 v[118:119], v8 offset:53760\ns_waitcnt lgkmcnt(6)\nv_mfma_f32_16x16x32_bf16 v[64:67], v[80:83], v[120:123], v[64:67]\n"
"v_mfma_f32_32x32x16_bf16 v[32:47], v[120:123], v[212:215], v[32:47]\ns_lshl_b32 s47, s44, 12\ns_add_u32 s47, s47, 0xa10000\n"
"v_lshl_add_u32 v86, v3, 7, s47\nv_mfma_f32_32x32x16_bf16 v[16:31], v[120:123], v[208:211], v[16:31]\ns_lshl_b32 s47, s42, 2\ns_and_b32 s47, s47, 24\n"
"v_mov_b32 v84, s47\nds_read_b64_tr_b16 v[120:121], v8 offset:51200\nds_read_b64_tr_b16 v[122:123], v8 offset:51712\ns_waitcnt lgkmcnt(6)\n"
"v_mfma_f32_16x16x32_bf16 v[64:67], v[80:83], v[124:127], v[64:67]\nv_mfma_f32_32x32x16_bf16 v[32:47], v[124:127], v[220:223], v[32:47]\n"
"v_mfma_f32_32x32x16_bf16 v[16:31], v[124:127], v[216:219], v[16:31]\nds_read_b64_tr_b16 v[124:125], v8 offset:55296\n"
"ds_read_b64_tr_b16 v[126:127], v8 offset:55808\ns_waitcnt lgkmcnt(6)\nv_mfma_f32_16x16x32_bf16 v[64:67], v[80:83], v[112:115], v[64:67]\n"
"v_mfma_f32_32x32x16_bf16 v[32:47], v[112:115], v[228:231], v[32:47]\nv_mfma_f32_32x32x16_bf16 v[16:31], v[112:115], v[224:227], v[16:31]\n"
"s_waitcnt lgkmcnt(4)\nv_mfma_f32_16x16x32_bf16 v[64:67], v[80:83], v[116:119], v[64:67]\n"
"v_mfma_f32_32x32x16_bf16 v[32:47], v[116:119], v[236:239], v[32:47]\nv_mfma_f32_32x32x16_bf16 v[16:31], v[116:119], v[232:235], v[16:31]\n"
"s_waitcnt lgkmcnt(2)\nv_mfma_f32_16x16x32_bf16 v[64:67], v[80:83], v[120:123], v[64:67]\ns_waitcnt lgkmcnt(0)\n"
"v_mfma_f32_16x16x32_bf16 v[64:67], v[80:83], v[124:127], v[64:67]\nv_mfma_f32_32x32x16_bf16 v[32:47], v[120:123], v[244:247], v[32:47]\ns_nop 4\n"
"v_cvt_pk_bf16_f32 v2, v64, v64\nv_and_b32 v2, 0xffff0000, v2\nv_sub_f32 v3, v64, v2\nv_cvt_pk_bf16_f32 v0, v64, v3\nv_and_b32 v2, 0xffff0000, v0\n"
"v_sub_f32 v3, v3, v2\nv_cvt_pk_bf16_f32 v1, v3, 0\nv_mfma_f32_32x32x16_bf16 v[32:47], v[124:127], v[252:255], v[32:47]\ns_nop 0\n"
"v_mfma_f32_32x32x8_bf16 v[32:47], v[0:1], v[110:111], v[32:47]\nv_mfma_f32_32x32x16_bf16 v[16:31], v[120:123], v[240:243], v[16:31]\n"
"v_mfma_f32_32x32x16_bf16 v[16:31], v[124:127], v[248:251], v[16:31]\ns_nop 1\nds_write_b128 v15, v[32:35] offset:32768\n"
"ds_write_b128 v15, v[36:39] offset:33792\nv_mfma_f32_32x32x8_bf16 v[16:31], v[0:1], v[104:105], v[16:31]\nds_write_b128 v15, v[40:43] offset:34816\n"
"ds_write_b128 v15, v[44:47] offset:35840\ns_waitcnt lgkmcnt(0)\ns_barrier\nds_read_b128 v[32:35], v108 offset:32768\n"
"ds_read_b128 v[36:39], v108 offset:33792\nds_read_b128 v[40:43], v108 offset:34816\nds_read_b128 v[44:47], v108 offset:35840\ns_waitcnt lgkmcnt(3)\n"
"s_nop 2\nv_pk_add_f32 v[16:17], v[16:17], v[32:33]\nv_pk_add_f32 v[18:19], v[18:19], v[34:35]\ns_waitcnt lgkmcnt(2)\n"
"v_pk_add_f32 v[20:21], v[20:21], v[36:37]\nv_pk_add_f32 v[22:23], v[22:23], v[38:39]\n"
"v_pk_mul_f32 v[16:17], v[16:17], v[106:107] op_sel:[0,0] op_sel_hi:[1,0]\nv_pk_mul_f32 v[18:19], v[18:19], v[106:107] op_sel:[0,0] op_sel_hi:[1,0]\n"
"v_pk_mul_f32 v[20:21], v[20:21], v[106:107] op_sel:[0,0] op_sel_hi:[1,0]\nv_pk_mul_f32 v[22:23], v[22:23], v[106:107] op_sel:[0,0] op_sel_hi:[1,0]\n"
"v_cvt_pk_bf16_f32 v16, v16, v17\nv_cvt_pk_bf16_f32 v17, v18, v19\nv_cvt_pk_bf16_f32 v18, v20, v21\nv_cvt_pk_bf16_f32 v19, v22, v23\n"
"s_add_u32 s53, s62, 0x200000\nbuffer_store_dwordx4 v[16:19], v11, s[12:15], s53 offen sc1\ns_waitcnt lgkmcnt(1)\n"
"v_pk_add_f32 v[24:25], v[24:25], v[40:41]\nv_pk_add_f32 v[26:27], v[26:27], v[42:43]\ns_waitcnt lgkmcnt(0)\n"
"v_pk_add_f32 v[28:29], v[28:29], v[44:45]\nv_pk_add_f32 v[30:31], v[30:31], v[46:47]\n"
"v_pk_mul_f32 v[24:25], v[24:25], v[106:107] op_sel:[0,0] op_sel_hi:[1,0]\nv_pk_mul_f32 v[26:27], v[26:27], v[106:107] op_sel:[0,0] op_sel_hi:[1,0]\n"
"v_pk_mul_f32 v[28:29], v[28:29], v[106:107] op_sel:[0,0] op_sel_hi:[1,0]\nv_pk_mul_f32 v[30:31], v[30:31], v[106:107] op_sel:[0,0] op_sel_hi:[1,0]\n"
"v_cvt_pk_bf16_f32 v24, v24, v25\nv_cvt_pk_bf16_f32 v25, v26, v27\nv_cvt_pk_bf16_f32 v26, v28, v29\nv_cvt_pk_bf16_f32 v27, v30, v31\n"
"s_add_u32 s53, s62, 0x210000\nbuffer_store_dwordx4 v[24:27], v11, s[12:15], s53 offen sc1\ns_nop 1\ns_branch .Ljoin\n.Lmv:\ns_nop 1\ns_barrier\n"
"s_add_u32 s40, s40, 0x9e3779b9\ns_xor_b32 s41, s41, 0x85ebca6b\nds_read_b64_tr_b16 v[112:113], v9 offset:0\n"
"ds_read_b64_tr_b16 v[114:115], v9 offset:512\nds_read_b128 v[132:135], v4 offset:4096\nds_read_b128 v[128:131], v4 offset:0\n"
"ds_read_b64_tr_b16 v[116:117], v9 offset:4096\nds_read_b64_tr_b16 v[118:119], v9 offset:4608\nds_read_b128 v[140:143], v6 offset:4096\n"
"ds_read_b128 v[136:139], v6 offset:0\nds_read_b128 v[148:151], v5 offset:4096\nds_read_b128 v[144:147], v5 offset:0\n"
"ds_read_b128 v[156:159], v7 offset:4096\nds_read_b128 v[152:155], v7 offset:0\nds_read_b64_tr_b16 v[120:121], v9 offset:2048\n"
"ds_read_b64_tr_b16 v[122:123], v9 offset:2560\nds_read_b64_tr_b16 v[124:125], v9 offset:6144\nds_read_b64_tr_b16 v[126:127], v9 offset:6656\n"
"s_waitcnt lgkmcnt(14)\nv_mfma_f32_16x16x32_bf16 v[64:67], v[80:83], v[112:115], 0\ns_waitcnt lgkmcnt(13)\n"
"v_mfma_f32_32x32x16_bf16 v[32:47], v[132:135], v[112:115], 0\ns_waitcnt lgkmcnt(12)\nv_mfma_f32_32x32x16_bf16 v[16:31], v[128:131], v[112:115], 0\n"
"s_waitcnt lgkmcnt(10)\nv_mfma_f32_16x16x32_bf16 v[64:67], v[80:83], v[116:119], v[64:67]\ns_waitcnt lgkmcnt(9)\n"
"v_mfma_f32_32x32x16_bf16 v[32:47], v[140:143], v[116:119], v[32:47]\ns_waitcnt lgkmcnt(8)\n"
"v_mfma_f32_32x32x16_bf16 v[16:31], v[136:139], v[116:119], v[16:31]\ns_waitcnt lgkmcnt(4)\ns_waitcnt lgkmcnt(0)\ns_barrier\n"
"ds_read_b64_tr_b16 v[112:113], v9 offset:16384\nds_read_b64_tr_b16 v[114:115], v9 offset:16896\nds_read_b64_tr_b16 v[116:117], v9 offset:20480\n"
"ds_read_b64_tr_b16 v[118:119], v9 offset:20992\nv_mfma_f32_16x16x32_bf16 v[64:67], v[80:83], v[120:123], v[64:67]\n"
"v_mfma_f32_32x32x16_bf16 v[32:47], v[148:151], v[120:123], v[32:47]\nds_read_b128 v[164:167], v4 offset:20480\n"
"ds_read_b128 v[160:163], v4 offset:16384\nv_mfma_f32_32x32x16_bf16 v[16:31], v[144:147], v[120:123], v[16:31]\n"
"ds_read_b128 v[172:175], v6 offset:20480\nds_read_b128 v[168:171], v6 offset:16384\nds_read_b64_tr_b16 v[120:121], v9 offset:18432\n"
"ds_read_b64_tr_b16 v[122:123], v9 offset:18944\nv_mfma_f32_16x16x32_bf16 v[64:67], v[80:83], v[124:127], v[64:67]\n"
"v_mfma_f32_32x32x16_bf16 v[32:47], v[156:159], v[124:127], v[32:47]\nds_read_b128 v[180:183], v5 offset:20480\n"
"ds_read_b128 v[176:179], v5 offset:16384\nv_mfma_f32_32x32x16_bf16 v[16:31], v[152:155], v[124:127], v[16:31]\n"
"ds_read_b128 v[188:191], v7 offset:20480\nds_read_b128 v[184:187], v7 offset:16384\nds_read_b64_tr_b16 v[124:125], v9 offset:22528\n"
"ds_read_b64_tr_b16 v[126:127], v9 offset:23040\ns_waitcnt lgkmcnt(14)\nv_mfma_f32_16x16x32_bf16 v[64:67], v[80:83], v[112:115], v[64:67]\n"
"s_waitcnt lgkmcnt(11)\nv_mfma_f32_32x32x16_bf16 v[32:47], v[164:167], v[112:115], v[32:47]\nds_read_b128 v[196:199], v4 offset:36864\n"
"ds_read_b128 v[192:195], v4 offset:32768\ns_waitcnt lgkmcnt(12)\nv_mfma_f32_32x32x16_bf16 v[16:31], v[160:163], v[112:115], v[16:31]\n"
"ds_read_b128 v[204:207], v6 offset:36864\nds_read_b128 v[200:203], v6 offset:32768\n"
"v_mfma_f32_16x16x32_bf16 v[64:67], v[80:83], v[116:119], v[64:67]\ns_waitcnt lgkmcnt(13)\n"
"v_mfma_f32_32x32x16_bf16 v[32:47], v[172:175], v[116:119], v[32:47]\nds_read_b128 v[212:215], v5 offset:36864\n"
"ds_read_b128 v[208:211], v5 offset:32768\ns_waitcnt lgkmcnt(14)\nv_mfma_f32_32x32x16_bf16 v[16:31], v[168:171], v[116:119], v[16:31]\n"
"ds_read_b128 v[220:223], v7 offset:36864\nds_read_b128 v[216:219], v7 offset:32768\ns_waitcnt lgkmcnt(10)\ns_waitcnt lgkmcnt(8)\ns_barrier\n"
"ds_read_b64_tr_b16 v[112:113], v9 offset:32768\nds_read_b64_tr_b16 v[114:115], v9 offset:33280\nds_read_b64_tr_b16 v[116:117], v9 offset:36864\n"
"ds_read_b64_tr_b16 v[118:119], v9 offset:37376\nv_mfma_f32_16x16x32_bf16 v[64:67], v[80:83], v[120:123], v[64:67]\n"
"v_mfma_f32_32x32x16_bf16 v[32:47], v[180:183], v[120:123], v[32:47]\nds_read_b128 v[228:231], v4 offset:53248\n"
"ds_read_b128 v[224:227], v4 offset:49152\nv_mfma_f32_32x32x16_bf16 v[16:31], v[176:179], v[120:123], v[16:31]\n"
"ds_read_b128 v[236:239], v6 offset:53248\nds_read_b128 v[232:235], v6 offset:49152\nds_read_b64_tr_b16 v[120:121], v9 offset:34816\n"
"ds_read_b64_tr_b16 v[122:123], v9 offset:35328\nv_mfma_f32_16x16x32_bf16 v[64:67], v[80:83], v[124:127], v[64:67]\n"
"v_mfma_f32_32x32x16_bf16 v[32:47], v[188:191], v[124:127], v[32:47]\nds_read_b128 v[244:247], v5 offset:53248\n"
"ds_read_b128 v[240:243], v5 offset:49152\nv_mfma_f32_32x32x16_bf16 v[16:31], v[184:187], v[124:127], v[16:31]\n"
"ds_read_b128 v[252:255], v7 offset:53248\nds_read_b128 v[248:251], v7 offset:49152\nds_read_b64_tr_b16 v[124:125], v9 offset:38912\n"
"ds_read_b64_tr_b16 v[126:127], v9 offset:39424\ns_waitcnt lgkmcnt(14)\nv_mfma_f32_16x16x32_bf16 v[64:67], v[80:83], v[112:115], v[64:67]\n"
"v_mfma_f32_32x32x16_bf16 v[32:47], v[196:199], v[112:115], v[32:47]\nv_mfma_f32_32x32x16_bf16 v[16:31], v[192:195], v[112:115], v[16:31]\n"
"s_waitcnt lgkmcnt(12)\nv_mfma_f32_16x16x32_bf16 v[64:67], v[80:83], v[116:119], v[64:67]\n"
"v_mfma_f32_32x32x16_bf16 v[32:47], v[204:207], v[116:119], v[32:47]\nv_mfma_f32_32x32x16_bf16 v[16:31], v[200:203], v[116:119], v[16:31]\n"
"s_waitcnt lgkmcnt(2)\ns_waitcnt lgkmcnt(0)\ns_barrier\nds_read_b64_tr_b16 v[112:113], v9 offset:49152\n"
"ds_read_b64_tr_b16 v[114:115], v9 offset:49664\nds_read_b64_tr_b16 v[116:117], v9 offset:53248\nds_read_b64_tr_b16 v[118:119], v9 offset:53760\n"
"v_mfma_f32_16x16x32_bf16 v[64:67], v[80:83], v[120:123], v[64:67]\nv_mfma_f32_32x32x16_bf16 v[32:47], v[212:215], v[120:123], v[32:47]\n"
"v_mfma_f32_32x32x16_bf16 v[16:31], v[208:211], v[120:123], v[16:31]\nds_read_b64_tr_b16 v[120:121], v9 offset:51200\n"
"ds_read_b64_tr_b16 v[122:123], v9 offset:51712\nv_mfma_f32_16x16x32_bf16 v[64:67], v[80:83], v[124:127], v[64:67]\n"
"v_mfma_f32_32x32x16_bf16 v[32:47], v[220:223], v[124:127], v[32:47]\nv_mfma_f32_32x32x16_bf16 v[16:31], v[216:219], v[124:127], v[16:31]\n"
"ds_read_b64_tr_b16 v[124:125], v9 offset:55296\nds_read_b64_tr_b16 v[126:127], v9 offset:55808\ns_waitcnt lgkmcnt(6)\n"
"v_mfma_f32_16x16x32_bf16 v[64:67], v[80:83], v[112:115], v[64:67]\nv_mfma_f32_32x32x16_bf16 v[32:47], v[228:231], v[112:115], v[32:47]\n"
"v_mfma_f32_32x32x16_bf16 v[16:31], v[224:227], v[112:115], v[16:31]\ns_waitcnt lgkmcnt(4)\n"
"v_mfma_f32_16x16x32_bf16 v[64:67], v[80:83], v[116:119], v[64:67]\nv_mfma_f32_32x32x16_bf16 v[32:47], v[236:239], v[116:119], v[32:47]\n"
"v_mfma_f32_32x32x16_bf16 v[16:31], v[232:235], v[116:119], v[16:31]\ns_waitcnt lgkmcnt(0)\ns_barrier\nds_read_b64_tr_b16 v[112:113], v10 offset:0\n"
"ds_read_b64_tr_b16 v[114:115], v10 offset:512\nds_read_b64_tr_b16 v[116:117], v10 offset:4096\nds_read_b64_tr_b16 v[118:119], v10 offset:4608\n"
"ds_read_b128 v[104:107], v12 offset:16384\nds_read_b64 v[110:111], v109 offset:16384\n"
"v_mfma_f32_16x16x32_bf16 v[64:67], v[80:83], v[120:123], v[64:67]\nv_mfma_f32_32x32x16_bf16 v[32:47], v[244:247], v[120:123], v[32:47]\n"
"v_mfma_f32_32x32x16_bf16 v[16:31], v[240:243], v[120:123], v[16:31]\nds_read_b64_tr_b16 v[120:121], v10 offset:2048\n"
"ds_read_b64_tr_b16 v[122:123], v10 offset:2560\nv_mfma_f32_16x16x32_bf16 v[64:67], v[80:83], v[124:127], v[64:67]\n"
"v_mfma_f32_32x32x16_bf16 v[32:47], v[252:255], v[124:127], v[32:47]\nv_mfma_f32_32x32x16_bf16 v[16:31], v[248:251], v[124:127], v[16:31]\n"
"ds_read_b64_tr_b16 v[124:125], v10 offset:6144\nds_read_b64_tr_b16 v[126:127], v10 offset:6656\ns_waitcnt lgkmcnt(8)\n"
"v_mfma_f32_16x16x32_bf16 v[68:71], v[80:83], v[112:115], 0\nv_mfma_f32_32x32x16_bf16 v[88:103], v[112:115], v[132:135], 0\n"
"v_cvt_pk_bf16_f32 v2, v64, v64\nv_and_b32 v2, 0xffff0000, v2\nv_sub_f32 v3, v64, v2\nv_cvt_pk_bf16_f32 v0, v64, v3\nv_and_b32 v2, 0xffff0000, v0\n"
"v_sub_f32 v3, v3, v2\nv_cvt_pk_bf16_f32 v1, v3, 0\ns_waitcnt lgkmcnt(4)\ns_nop 0\nv_mfma_f32_32x32x8_bf16 v[32:47], v[110:111], v[0:1], v[32:47]\n"
"v_mfma_f32_32x32x16_bf16 v[48:63], v[112:115], v[128:131], 0\nv_mfma_f32_32x32x8_bf16 v[16:31], v[104:105], v[0:1], v[16:31]\n"
"v_mfma_f32_16x16x32_bf16 v[68:71], v[80:83], v[116:119], v[68:71]\nds_write_b128 v15, v[32:35] offset:32768\n"
"v_mfma_f32_32x32x16_bf16 v[88:103], v[116:119], v[140:143], v[88:103]\nds_write_b128 v15, v[36:39] offset:33792\n"
"ds_write_b128 v15, v[40:43] offset:34816\nv_mfma_f32_32x32x16_bf16 v[48:63], v[116:119], v[136:139], v[48:63]\n"
"ds_write_b128 v15, v[44:47] offset:35840\ns_waitcnt lgkmcnt(4)\ns_waitcnt lgkmcnt(0)\ns_barrier\nds_read_b64_tr_b16 v[112:113], v10 offset:16384\n"
"ds_read_b64_tr_b16 v[114:115], v10 offset:16896\nds_read_b64_tr_b16 v[116:117], v10 offset:20480\nds_read_b64_tr_b16 v[118:119], v10 offset:20992\n"
"ds_read_b128 v[32:35], v108 offset:32768\nds_read_b128 v[36:39], v108 offset:33792\nds_read_b128 v[40:43], v108 offset:34816\n"
"ds_read_b128 v[44:47], v108 offset:35840\nds_read_b128 v[72:75], v13 offset:17408\nds_read_b128 v[76:79], v13 offset:17440\n"
"v_mfma_f32_16x16x32_bf16 v[68:71], v[80:83], v[120:123], v[68:71]\nv_mfma_f32_32x32x16_bf16 v[88:103], v[120:123], v[148:151], v[88:103]\n"
"v_mfma_f32_32x32x16_bf16 v[48:63], v[120:123], v[144:147], v[48:63]\nds_read_b64_tr_b16 v[120:121], v10 offset:18432\n"
"ds_read_b64_tr_b16 v[122:123], v10 offset:18944\nv_mfma_f32_16x16x32_bf16 v[68:71], v[80:83], v[124:127], v[68:71]\ns_waitcnt lgkmcnt(7)\n"
"v_pk_add_f32 v[16:17], v[16:17], v[32:33]\nv_pk_add_f32 v[18:19], v[18:19], v[34:35]\ns_waitcnt lgkmcnt(6)\n"
"v_pk_add_f32 v[20:21], v[20:21], v[36:37]\nv_pk_add_f32 v[22:23], v[22:23], v[38:39]\nds_read_b128 v[32:35], v13 offset:17472\n"
"ds_read_b128 v[36:39], v13 offset:17504\nv_mfma_f32_32x32x16_bf16 v[88:103], v[124:127], v[156:159], v[88:103]\ns_waitcnt lgkmcnt(5)\n"
"v_pk_mul_f32 v[16:17], v[16:17], v[72:73]\nv_pk_mul_f32 v[18:19], v[18:19], v[74:75]\ns_waitcnt lgkmcnt(4)\n"
"v_pk_mul_f32 v[20:21], v[20:21], v[76:77]\nv_pk_mul_f32 v[22:23], v[22:23], v[78:79]\n"
"v_mfma_f32_32x32x16_bf16 v[48:63], v[124:127], v[152:155], v[48:63]\nv_cvt_pk_bf16_f32 v16, v16, v17\nv_cvt_pk_bf16_f32 v17, v18, v19\n"
"v_cvt_pk_bf16_f32 v18, v20, v21\nv_cvt_pk_bf16_f32 v19, v22, v23\ns_add_u32 s53, s62, 0x400000\n"
"buffer_store_dwordx4 v[16:19], v11, s[12:15], s53 offen offset:0 sc1\nds_read_b64_tr_b16 v[124:125], v10 offset:22528\n"
"ds_read_b64_tr_b16 v[126:127], v10 offset:23040\nv_mfma_f32_16x16x32_bf16 v[68:71], v[80:83], v[112:115], v[68:71]\n"
"v_pk_add_f32 v[24:25], v[24:25], v[40:41]\nv_pk_add_f32 v[26:27], v[26:27], v[42:43]\nv_pk_add_f32 v[28:29], v[28:29], v[44:45]\n"
"v_pk_add_f32 v[30:31], v[30:31], v[46:47]\nv_mfma_f32_32x32x16_bf16 v[88:103], v[112:115], v[164:167], v[88:103]\ns_waitcnt lgkmcnt(3)\n"
"v_pk_mul_f32 v[24:25], v[24:25], v[32:33]\nv_pk_mul_f32 v[26:27], v[26:27], v[34:35]\ns_waitcnt lgkmcnt(2)\n"
"v_pk_mul_f32 v[28:29], v[28:29], v[36:37]\nv_pk_mul_f32 v[30:31], v[30:31], v[38:39]\n"
"v_mfma_f32_32x32x16_bf16 v[48:63], v[112:115], v[160:163], v[48:63]\nv_cvt_pk_bf16_f32 v24, v24, v25\nv_cvt_pk_bf16_f32 v25, v26, v27\n"
"v_cvt_pk_bf16_f32 v26, v28, v29\nv_cvt_pk_bf16_f32 v27, v30, v31\ns_add_u32 s53, s62, 0x400000\n"
"buffer_store_dwordx4 v[24:27], v11, s[12:15], s53 offen offset:512 sc1\nv_mfma_f32_16x16x32_bf16 v[68:71], v[80:83], v[116:119], v[68:71]\n"
"v_mfma_f32_32x32x16_bf16 v[88:103], v[116:119], v[172:175], v[88:103]\nv_mov_b32 v73, s40\nv_mov_b32 v75, s40\nv_mov_b32 v77, s40\n"
"v_mov_b32 v79, s40\nv_mfma_f32_32x32x16_bf16 v[48:63], v[116:119], v[168:171], v[48:63]\ns_waitcnt lgkmcnt(0)\ns_barrier\n"
"ds_read_b64_tr_b16 v[112:113], v9 offset:0\nds_read_b64_tr_b16 v[114:115], v9 offset:512\nds_read_b64_tr_b16 v[116:117], v9 offset:4096\n"
"ds_read_b64_tr_b16 v[118:119], v9 offset:4608\nv_mfma_f32_16x16x32_bf16 v[68:71], v[80:83], v[120:123], v[68:71]\n"
"v_mfma_f32_32x32x16_bf16 v[88:103], v[120:123], v[180:183], v[88:103]\nv_mfma_f32_32x32x16_bf16 v[48:63], v[120:123], v[176:179], v[48:63]\n"
"ds_read_b64_tr_b16 v[120:121], v9 offset:2048\nds_read_b64_tr_b16 v[122:123], v9 offset:2560\n"
"v_mfma_f32_16x16x32_bf16 v[68:71], v[80:83], v[124:127], v[68:71]\nv_mfma_f32_32x32x16_bf16 v[88:103], v[124:127], v[188:191], v[88:103]\n"
"v_mfma_f32_32x32x16_bf16 v[48:63], v[124:127], v[184:187], v[48:63]\nds_read_b64_tr_b16 v[124:125], v9 offset:6144\n"
"ds_read_b64_tr_b16 v[126:127], v9 offset:6656\ns_waitcnt lgkmcnt(6)\nv_mfma_f32_16x16x32_bf16 v[68:71], v[80:83], v[112:115], v[68:71]\n"
"v_mfma_f32_32x32x16_bf16 v[88:103], v[112:115], v[196:199], v[88:103]\nv_mfma_f32_32x32x16_bf16 v[48:63], v[112:115], v[192:195], v[48:63]\n"
"s_waitcnt lgkmcnt(4)\nv_mfma_f32_16x16x32_bf16 v[68:71], v[80:83], v[116:119], v[68:71]\n"
"v_mfma_f32_32x32x16_bf16 v[88:103], v[116:119], v[204:207], v[88:103]\nv_mfma_f32_32x32x16_bf16 v[48:63], v[116:119], v[200:203], v[48:63]\n"
"s_waitcnt lgkmcnt(0)\ns_waitcnt vmcnt(0)\ns_barrier\nds_read_b64_tr_b16 v[112:113], v8 offset:0\nds_read_b64_tr_b16 v[114:115], v8 offset:512\n"
"ds_read_b64_tr_b16 v[116:117], v8 offset:4096\nds_read_b64_tr_b16 v[118:119], v8 offset:4608\n"
"v_mfma_f32_16x16x32_bf16 v[68:71], v[80:83], v[120:123], v[68:71]\nv_mfma_f32_32x32x16_bf16 v[88:103], v[120:123], v[212:215], v[88:103]\n"
"v_mfma_f32_32x32x16_bf16 v[48:63], v[120:123], v[208:211], v[48:63]\nds_read_b64_tr_b16 v[120:121], v8 offset:2048\n"
"ds_read_b64_tr_b16 v[122:123], v8 offset:2560\nv_mfma_f32_16x16x32_bf16 v[68:71], v[80:83], v[124:127], v[68:71]\n"
"v_mfma_f32_32x32x16_bf16 v[88:103], v[124:127], v[220:223], v[88:103]\nv_mfma_f32_32x32x16_bf16 v[48:63], v[124:127], v[216:219], v[48:63]\n"
"ds_read_b64_tr_b16 v[124:125], v8 offset:6144\nds_read_b64_tr_b16 v[126:127], v8 offset:6656\ns_waitcnt lgkmcnt(6)\n"
"v_mfma_f32_16x16x32_bf16 v[68:71], v[80:83], v[112:115], v[68:71]\nv_mfma_f32_32x32x16_bf16 v[88:103], v[112:115], v[228:231], v[88:103]\n"
"v_mfma_f32_32x32x16_bf16 v[48:63], v[112:115], v[224:227], v[48:63]\ns_waitcnt lgkmcnt(4)\n"
"v_mfma_f32_16x16x32_bf16 v[68:71], v[80:83], v[116:119], v[68:71]\nv_mfma_f32_32x32x16_bf16 v[88:103], v[116:119], v[236:239], v[88:103]\n"
"v_mfma_f32_32x32x16_bf16 v[48:63], v[116:119], v[232:235], v[48:63]\ns_waitcnt lgkmcnt(2)\n"
"v_mfma_f32_16x16x32_bf16 v[68:71], v[80:83], v[120:123], v[68:71]\ns_waitcnt lgkmcnt(0)\n"
"v_mfma_f32_16x16x32_bf16 v[68:71], v[80:83], v[124:127], v[68:71]\nv_mfma_f32_32x32x16_bf16 v[88:103], v[120:123], v[244:247], v[88:103]\ns_nop 4\n"
"v_cvt_pk_bf16_f32 v2, v68, v68\nv_and_b32 v2, 0xffff0000, v2\nv_sub_f32 v3, v68, v2\nv_cvt_pk_bf16_f32 v0, v68, v3\nv_and_b32 v2, 0xffff0000, v0\n"
"v_sub_f32 v3, v3, v2\nv_cvt_pk_bf16_f32 v1, v3, 0\nv_mfma_f32_32x32x16_bf16 v[88:103], v[124:127], v[252:255], v[88:103]\ns_nop 0\n"
"v_mfma_f32_32x32x8_bf16 v[88:103], v[0:1], v[110:111], v[88:103]\nv_mfma_f32_32x32x16_bf16 v[48:63], v[120:123], v[240:243], v[48:63]\n"
"v_mfma_f32_32x32x16_bf16 v[48:63], v[124:127], v[248:251], v[48:63]\ns_nop 1\nds_write_b128 v15, v[88:91] offset:32768\n"
"ds_write_b128 v15, v[92:95] offset:33792\nv_mfma_f32_32x32x8_bf16 v[48:63], v[0:1], v[104:105], v[48:63]\nds_write_b128 v15, v[96:99] offset:34816\n"
"ds_write_b128 v15, v[100:103] offset:35840\ns_waitcnt lgkmcnt(0)\ns_barrier\nds_read_b128 v[88:91], v108 offset:32768\n"
"ds_read_b128 v[92:95], v108 offset:33792\nds_read_b128 v[96:99], v108 offset:34816\nds_read_b128 v[100:103], v108 offset:35840\n"
"ds_read_b64_tr_b16 v[112:113], v9 offset:16384\nds_read_b64_tr_b16 v[114:115], v9 offset:16896\nds_read_b64_tr_b16 v[116:117], v9 offset:20480\n"
"ds_read_b64_tr_b16 v[118:119], v9 offset:20992\nds_read_b64_tr_b16 v[120:121], v9 offset:18432\nds_read_b64_tr_b16 v[122:123], v9 offset:18944\n"
"ds_read_b64_tr_b16 v[124:125], v9 offset:22528\nds_read_b64_tr_b16 v[126:127], v9 offset:23040\ns_waitcnt lgkmcnt(11)\n"
"v_pk_add_f32 v[48:49], v[48:49], v[88:89]\nv_pk_add_f32 v[50:51], v[50:51], v[90:91]\ns_waitcnt lgkmcnt(10)\n"
"v_pk_add_f32 v[52:53], v[52:53], v[92:93]\nv_pk_add_f32 v[54:55], v[54:55], v[94:95]\ns_waitcnt lgkmcnt(6)\n"
"v_mfma_f32_16x16x32_bf16 v[64:67], v[80:83], v[112:115], 0\nv_pk_mul_f32 v[48:49], v[48:49], v[106:107] op_sel:[0,1] op_sel_hi:[1,1]\n"
"v_pk_mul_f32 v[50:51], v[50:51], v[106:107] op_sel:[0,1] op_sel_hi:[1,1]\nv_pk_mul_f32 v[52:53], v[52:53], v[106:107] op_sel:[0,1] op_sel_hi:[1,1]\n"
"v_pk_mul_f32 v[54:55], v[54:55], v[106:107] op_sel:[0,1] op_sel_hi:[1,1]\nv_mfma_f32_32x32x16_bf16 v[32:47], v[132:135], v[112:115], 0\n"
"s_add_u32 s53, s63, 0x1800000\nv_cvt_pk_bf16_f32 v72, v48, v49\nv_cvt_pk_bf16_f32 v74, v52, v53\n"
"buffer_store_dwordx4 v[72:75], v87, s[12:15], s53 offen sc1\nv_cvt_pk_bf16_f32 v76, v50, v51\nv_cvt_pk_bf16_f32 v78, v54, v55\n"
"buffer_store_dwordx4 v[76:79], v87, s[12:15], s53 offen offset:1024 sc1\nv_mfma_f32_32x32x16_bf16 v[16:31], v[128:131], v[112:115], 0\n"
"v_pk_add_f32 v[56:57], v[56:57], v[96:97]\nv_pk_add_f32 v[58:59], v[58:59], v[98:99]\nv_pk_add_f32 v[60:61], v[60:61], v[100:101]\n"
"v_pk_add_f32 v[62:63], v[62:63], v[102:103]\nv_pk_mul_f32 v[56:57], v[56:57], v[106:107] op_sel:[0,1] op_sel_hi:[1,1]\n"
"v_pk_mul_f32 v[58:59], v[58:59], v[106:107] op_sel:[0,1] op_sel_hi:[1,1]\nv_pk_mul_f32 v[60:61], v[60:61], v[106:107] op_sel:[0,1] op_sel_hi:[1,1]\n"
"v_pk_mul_f32 v[62:63], v[62:63], v[106:107] op_sel:[0,1] op_sel_hi:[1,1]\ns_waitcnt lgkmcnt(4)\n"
"v_mfma_f32_16x16x32_bf16 v[64:67], v[80:83], v[116:119], v[64:67]\ns_add_u32 s53, s63, 0x1820000\nv_cvt_pk_bf16_f32 v72, v56, v57\n"
"v_cvt_pk_bf16_f32 v74, v60, v61\nbuffer_store_dwordx4 v[72:75], v87, s[12:15], s53 offen sc1\nv_cvt_pk_bf16_f32 v76, v58, v59\n"
"v_cvt_pk_bf16_f32 v78, v62, v63\nbuffer_store_dwordx4 v[76:79], v87, s[12:15], s53 offen offset:1024 sc1\n"
"v_mfma_f32_32x32x16_bf16 v[32:47], v[140:143], v[116:119], v[32:47]\nv_mfma_f32_32x32x16_bf16 v[16:31], v[136:139], v[116:119], v[16:31]\ns_barrier\n"
"ds_read_b64_tr_b16 v[112:113], v9 offset:32768\nds_read_b64_tr_b16 v[114:115], v9 offset:33280\nds_read_b64_tr_b16 v[116:117], v9 offset:36864\n"
"ds_read_b64_tr_b16 v[118:119], v9 offset:37376\ns_waitcnt lgkmcnt(6)\nv_mfma_f32_16x16x32_bf16 v[64:67], v[80:83], v[120:123], v[64:67]\n"
"v_mfma_f32_32x32x16_bf16 v[32:47], v[148:151], v[120:123], v[32:47]\nv_mfma_f32_32x32x16_bf16 v[16:31], v[144:147], v[120:123], v[16:31]\n"
"ds_read_b64_tr_b16 v[120:121], v9 offset:34816\nds_read_b64_tr_b16 v[122:123], v9 offset:35328\ns_waitcnt lgkmcnt(6)\n"
"v_mfma_f32_16x16x32_bf16 v[64:67], v[80:83], v[124:127], v[64:67]\nv_mfma_f32_32x32x16_bf16 v[32:47], v[156:159], v[124:127], v[32:47]\n"
"v_lshrrev_b32 v2, 4, v87\ns_xor_b32 s62, s62, 0x40000\nv_lshrrev_b32 v4, 2, v2\nv_mfma_f32_32x32x16_bf16 v[16:31], v[152:155], v[124:127], v[16:31]\n"
"v_and_b32 v5, 3, v2\nv_lshlrev_b32 v5, 4, v5\nv_xor_b32 v6, s3, v5\nds_read_b64_tr_b16 v[124:125], v9 offset:38912\n"
"ds_read_b64_tr_b16 v[126:127], v9 offset:39424\ns_waitcnt lgkmcnt(6)\nv_mfma_f32_16x16x32_bf16 v[64:67], v[80:83], v[112:115], v[64:67]\n"
"v_mfma_f32_32x32x16_bf16 v[32:47], v[164:167], v[112:115], v[32:47]\nv_mad_u32_u24 v100, v4, s23, v6\nv_xor_b32 v6, s52, v5\n"
"v_lshl_add_u32 v101, v4, 10, v6\nv_mfma_f32_32x32x16_bf16 v[16:31], v[160:163], v[112:115], v[16:31]\nv_lshrrev_b32 v4, 5, v2\n"
"v_bfe_u32 v5, v2, 2, 2\nv_lshl_add_u32 v6, v4, 2, v5\nds_read_b64_tr_b16 v[112:113], v9 offset:49152\nds_read_b64_tr_b16 v[114:115], v9 offset:49664\n"
"s_waitcnt lgkmcnt(6)\nv_mfma_f32_16x16x32_bf16 v[64:67], v[80:83], v[116:119], v[64:67]\n"
"v_mfma_f32_32x32x16_bf16 v[32:47], v[172:175], v[116:119], v[32:47]\nv_lshlrev_b32 v6, 6, v6\nv_bfe_u32 v5, v2, 4, 1\nv_lshl_add_u32 v6, v5, 5, v6\n"
"v_mfma_f32_32x32x16_bf16 v[16:31], v[168:171], v[116:119], v[16:31]\nv_and_b32 v3, 3, v2\nv_lshl_add_u32 v6, v3, 3, v6\nv_add_u32 v102, s20, v6\n"
"ds_read_b64_tr_b16 v[116:117], v9 offset:53248\nds_read_b64_tr_b16 v[118:119], v9 offset:53760\ns_waitcnt lgkmcnt(6)\n"
"v_mfma_f32_16x16x32_bf16 v[64:67], v[80:83], v[120:123], v[64:67]\nv_mfma_f32_32x32x16_bf16 v[32:47], v[180:183], v[120:123], v[32:47]\n"
"v_and_b32 v3, 15, v2\nv_lshlrev_b32 v3, 4, v3\nv_lshl_add_u32 v3, v4, 8, v3\nv_mfma_f32_32x32x16_bf16 v[16:31], v[176:179], v[120:123], v[16:31]\n"
"v_add_u32 v6, 1, v5\nv_lshl_add_u32 v72, v6, 16, v3\nv_xor_b32 v5, 1, v5\nds_read_b64_tr_b16 v[120:121], v9 offset:51200\n"
"ds_read_b64_tr_b16 v[122:123], v9 offset:51712\ns_waitcnt lgkmcnt(6)\nv_mfma_f32_16x16x32_bf16 v[64:67], v[80:83], v[124:127], v[64:67]\n"
"v_mfma_f32_32x32x16_bf16 v[32:47], v[188:191], v[124:127], v[32:47]\nv_lshlrev_b32 v73, 12, v5\ns_lshl_b32 s48, s42, 10\nv_add_u32 v74, s48, v87\n"
"v_mfma_f32_32x32x16_bf16 v[16:31], v[184:187], v[124:127], v[16:31]\nv_add_u32 v75, 0x10000, v74\nv_and_b32 v3, 7, v2\nv_bfe_u32 v4, v2, 3, 1\n"
"ds_read_b64_tr_b16 v[124:125], v9 offset:55296\nds_read_b64_tr_b16 v[126:127], v9 offset:55808\ns_waitcnt lgkmcnt(6)\n"
"v_mfma_f32_16x16x32_bf16 v[64:67], v[80:83], v[112:115], v[64:67]\nv_mfma_f32_32x32x16_bf16 v[32:47], v[196:199], v[112:115], v[32:47]\n"
"v_mul_u32_u24 v4, 0x70000, v4\nv_lshl_add_u32 v3, v3, 9, v4\ns_lshr_b32 s47, s42, 1\n"
"v_mfma_f32_32x32x16_bf16 v[16:31], v[192:195], v[112:115], v[16:31]\ns_lshl_b32 s47, s47, 7\ns_lshr_b32 s48, s43, 2\ns_lshl_b32 s48, s48, 12\n"
"ds_read_b64_tr_b16 v[112:113], v8 offset:49152\nds_read_b64_tr_b16 v[114:115], v8 offset:49664\ns_waitcnt lgkmcnt(6)\n"
"v_mfma_f32_16x16x32_bf16 v[64:67], v[80:83], v[116:119], v[64:67]\nv_mfma_f32_32x32x16_bf16 v[32:47], v[204:207], v[116:119], v[32:47]\n"
"s_add_u32 s47, s47, s48\ns_add_u32 s47, s47, 0xa00000\nv_add_u32 v78, s47, v3\nv_mfma_f32_32x32x16_bf16 v[16:31], v[200:203], v[116:119], v[16:31]\n"
"s_cmp_eq_u32 s42, 7\ns_cselect_b32 s47, 0x40000000, 0\nv_add_u32 v79, s47, v87\nv_and_b32 v3, 31, v2\nds_read_b64_tr_b16 v[116:117], v8 offset:53248\n"
"ds_read_b64_tr_b16 v[118:119], v8 offset:53760\ns_waitcnt lgkmcnt(6)\nv_mfma_f32_16x16x32_bf16 v[64:67], v[80:83], v[120:123], v[64:67]\n"
"v_mfma_f32_32x32x16_bf16 v[32:47], v[212:215], v[120:123], v[32:47]\ns_lshl_b32 s47, s44, 12\ns_add_u32 s47, s47, 0xa10000\n"
"v_lshl_add_u32 v86, v3, 7, s47\nv_mfma_f32_32x32x16_bf16 v[16:31], v[208:211], v[120:123], v[16:31]\ns_lshl_b32 s47, s42, 2\ns_and_b32 s47, s47, 24\n"
"v_mov_b32 v84, s47\nds_read_b64_tr_b16 v[120:121], v8 offset:51200\nds_read_b64_tr_b16 v[122:123], v8 offset:51712\ns_waitcnt lgkmcnt(6)\n"
"v_mfma_f32_16x16x32_bf16 v[64:67], v[80:83], v[124:127], v[64:67]\nv_mfma_f32_32x32x16_bf16 v[32:47], v[220:223], v[124:127], v[32:47]\n"
"v_mfma_f32_32x32x16_bf16 v[16:31], v[216:219], v[124:127], v[16:31]\nds_read_b64_tr_b16 v[124:125], v8 offset:55296\n"
"ds_read_b64_tr_b16 v[126:127], v8 offset:55808\ns_waitcnt lgkmcnt(6)\nv_mfma_f32_16x16x32_bf16 v[64:67], v[80:83], v[112:115], v[64:67]\n"
"v_mfma_f32_32x32x16_bf16 v[32:47], v[228:231], v[112:115], v[32:47]\nv_mfma_f32_32x32x16_bf16 v[16:31], v[224:227], v[112:115], v[16:31]\n"
"s_waitcnt lgkmcnt(4)\nv_mfma_f32_16x16x32_bf16 v[64:67], v[80:83], v[116:119], v[64:67]\n"
"v_mfma_f32_32x32x16_bf16 v[32:47], v[236:239], v[116:119], v[32:47]\nv_mfma_f32_32x32x16_bf16 v[16:31], v[232:235], v[116:119], v[16:31]\n"
"s_waitcnt lgkmcnt(2)\nv_mfma_f32_16x16x32_bf16 v[64:67], v[80:83], v[120:123], v[64:67]\ns_waitcnt lgkmcnt(0)\n"
"v_mfma_f32_16x16x32_bf16 v[64:67], v[80:83], v[124:127], v[64:67]\nv_mfma_f32_32x32x16_bf16 v[32:47], v[244:247], v[120:123], v[32:47]\ns_nop 4\n"
"v_cvt_pk_bf16_f32 v2, v64, v64\nv_and_b32 v2, 0xffff0000, v2\nv_sub_f32 v3, v64, v2\nv_cvt_pk_bf16_f32 v0, v64, v3\nv_and_b32 v2, 0xffff0000, v0\n"
"v_sub_f32 v3, v3, v2\nv_cvt_pk_bf16_f32 v1, v3, 0\nv_mfma_f32_32x32x16_bf16 v[32:47], v[252:255], v[124:127], v[32:47]\ns_nop 0\n"
"v_mfma_f32_32x32x8_bf16 v[32:47], v[110:111], v[0:1], v[32:47]\nv_mfma_f32_32x32x16_bf16 v[16:31], v[240:243], v[120:123], v[16:31]\n"
"v_mfma_f32_32x32x16_bf16 v[16:31], v[248:251], v[124:127], v[16:31]\ns_nop 1\nds_write_b128 v15, v[32:35] offset:32768\n"
"ds_write_b128 v15, v[36:39] offset:33792\nv_mfma_f32_32x32x8_bf16 v[16:31], v[104:105], v[0:1], v[16:31]\nds_write_b128 v15, v[40:43] offset:34816\n"
"ds_write_b128 v15, v[44:47] offset:35840\nds_read_b128 v[128:131], v13 offset:17408\nds_read_b128 v[132:135], v13 offset:17440\n"
"ds_read_b128 v[136:139], v13 offset:17472\nds_read_b128 v[140:143], v13 offset:17504\ns_waitcnt lgkmcnt(0)\ns_barrier\n"
"ds_read_b128 v[32:35], v108 offset:32768\nds_read_b128 v[36:39], v108 offset:33792\nds_read_b128 v[40:43], v108 offset:34816\n"
"ds_read_b128 v[44:47], v108 offset:35840\ns_waitcnt lgkmcnt(3)\nv_pk_add_f32 v[16:17], v[16:17], v[32:33]\nv_pk_add_f32 v[18:19], v[18:19], v[34:35]\n"
"s_waitcnt lgkmcnt(2)\nv_pk_add_f32 v[20:21], v[20:21], v[36:37]\nv_pk_add_f32 v[22:23], v[22:23], v[38:39]\n"
"v_pk_mul_f32 v[16:17], v[16:17], v[128:129]\nv_pk_mul_f32 v[18:19], v[18:19], v[130:131]\nv_pk_mul_f32 v[20:21], v[20:21], v[132:133]\n"
"v_pk_mul_f32 v[22:23], v[22:23], v[134:135]\nv_cvt_pk_bf16_f32 v16, v16, v17\nv_cvt_pk_bf16_f32 v17, v18, v19\nv_cvt_pk_bf16_f32 v18, v20, v21\n"
"v_cvt_pk_bf16_f32 v19, v22, v23\ns_add_u32 s53, s62, 0x400000\nbuffer_store_dwordx4 v[16:19], v11, s[12:15], s53 offen offset:0 sc1\n"
"s_waitcnt lgkmcnt(1)\nv_pk_add_f32 v[24:25], v[24:25], v[40:41]\nv_pk_add_f32 v[26:27], v[26:27], v[42:43]\ns_waitcnt lgkmcnt(0)\n"
"v_pk_add_f32 v[28:29], v[28:29], v[44:45]\nv_pk_add_f32 v[30:31], v[30:31], v[46:47]\nv_pk_mul_f32 v[24:25], v[24:25], v[136:137]\n"
"v_pk_mul_f32 v[26:27], v[26:27], v[138:139]\nv_pk_mul_f32 v[28:29], v[28:29], v[140:141]\nv_pk_mul_f32 v[30:31], v[30:31], v[142:143]\n"
"v_cvt_pk_bf16_f32 v24, v24, v25\nv_cvt_pk_bf16_f32 v25, v26, v27\nv_cvt_pk_bf16_f32 v26, v28, v29\nv_cvt_pk_bf16_f32 v27, v30, v31\n"
"s_add_u32 s53, s62, 0x400000\nbuffer_store_dwordx4 v[24:27], v11, s[12:15], s53 offen offset:512 sc1\ns_nop 1\n.Ljoin:\n"
"ds_read_b128 v[68:71], v74 offset:20480\nds_read_b64 v[84:85], v84 offset:18432\nds_read_b128 v[56:59], v87 offset:0\n"
"ds_read_b128 v[60:63], v72 offset:0\ns_waitcnt lgkmcnt(1)\nv_mfma_f32_32x32x16_bf16 v[0:15], v[56:59], v[68:71], 0\n"
"ds_read_b128 v[56:59], v87 offset:1024\nv_mov_b32 v52, 0xdddddddd\nv_mov_b64 v[32:33], 0\nv_mov_b64 v[34:35], 0\nv_mov_b64 v[36:37], 0\n"
"v_mov_b64 v[38:39], 0\nv_mov_b64 v[40:41], 0\nv_mov_b64 v[42:43], 0\nv_mov_b64 v[44:45], 0\nv_mov_b64 v[46:47], 0\ns_nop 1\nv_exp_f32 v0, v0\n"
"v_exp_f32 v1, v1\nv_exp_f32 v2, v2\nv_exp_f32 v3, v3\ns_waitcnt lgkmcnt(0)\nds_read_b128 v[64:67], v72 offset:512\ns_setprio 3\n"
"v_readfirstlane_b32 s100, v84\nv_readfirstlane_b32 s3, v85\ns_xor_b32 s50, s50, 0x1000\ns_lshr_b32 s47, s100, 8\ns_and_b32 s100, s100, s47\n"
"s_bitcmp1_b32 s100, s3\ns_cbranch_scc0 .Lsl1\n.Lsr1:\ns_lshl_b32 s47, s3, 13\ns_add_u32 s3, s3, 2\ns_add_u32 s47, s47, s49\n"
"s_add_u32 s53, s47, 0x200000\nbuffer_load_dwordx4 v[136:139], v87, s[12:15], s47 offen sc1\n"
"buffer_load_dwordx4 v[140:143], v87, s[12:15], s53 offen sc1\nv_exp_f32 v4, v4\nv_exp_f32 v5, v5\nv_exp_f32 v6, v6\nv_exp_f32 v7, v7\n"
"v_mfma_f32_32x32x16_bf16 v[16:31], v[56:59], v[68:71], 0\nds_read_b128 v[56:59], v87 offset:2048\ns_add_u32 m0, s20, 0\ns_nop 0\n"
"buffer_load_dwordx4 v100, s[24:27], s21 offen lds\nv_exp_f32 v8, v8\nv_exp_f32 v9, v9\nv_exp_f32 v10, v10\nv_exp_f32 v11, v11\n"
"v_smfmac_f32_32x32x32_bf16 v[32:47], v[60:63], v[0:7], v52\nds_read_b128 v[60:63], v72 offset:1024\ns_add_u32 m0, s20, 1024\ns_nop 0\n"
"buffer_load_dwordx4 v101, s[28:31], s22 offen lds\nv_exp_f32 v12, v12\nv_exp_f32 v13, v13\nv_exp_f32 v14, v14\nv_exp_f32 v15, v15\n"
"v_exp_f32 v16, v16\nv_exp_f32 v17, v17\nv_exp_f32 v18, v18\nv_exp_f32 v19, v19\ns_waitcnt lgkmcnt(0)\n"
"v_smfmac_f32_32x32x32_bf16 v[32:47], v[64:67], v[8:15], v52\nds_read_b128 v[64:67], v72 offset:1536\nv_exp_f32 v20, v20\nv_exp_f32 v21, v21\n"
"v_exp_f32 v22, v22\nv_exp_f32 v23, v23\nv_mfma_f32_32x32x16_bf16 v[0:15], v[56:59], v[68:71], 0\nds_read_b128 v[56:59], v87 offset:3072\n"
"v_exp_f32 v24, v24\nv_exp_f32 v25, v25\nv_exp_f32 v26, v26\nv_exp_f32 v27, v27\nv_smfmac_f32_32x32x32_bf16 v[32:47], v[60:63], v[16:23], v52\n"
"ds_read_b128 v[60:63], v72 offset:2048\nv_exp_f32 v28, v28\nv_exp_f32 v29, v29\nv_exp_f32 v30, v30\nv_exp_f32 v31, v31\ns_nop 0\nv_exp_f32 v0, v0\n"
"v_exp_f32 v1, v1\nv_exp_f32 v2, v2\nv_exp_f32 v3, v3\ns_waitcnt lgkmcnt(0)\nv_smfmac_f32_32x32x32_bf16 v[32:47], v[64:67], v[24:31], v52\n"
"ds_read_b128 v[64:67], v72 offset:2560\nv_exp_f32 v4, v4\nv_exp_f32 v5, v5\nv_exp_f32 v6, v6\nv_exp_f32 v7, v7\n"
"v_mfma_f32_32x32x16_bf16 v[16:31], v[56:59], v[68:71], 0\nds_read_b128 v[56:59], v87 offset:4096\nv_exp_f32 v8, v8\nv_exp_f32 v9, v9\n"
"v_exp_f32 v10, v10\nv_exp_f32 v11, v11\nv_smfmac_f32_32x32x32_bf16 v[32:47], v[60:63], v[0:7], v52\nds_read_b128 v[60:63], v72 offset:3072\n"
"buffer_load_dwordx2 v[76:77], v78, s[12:15], 0 offen sc1\nv_exp_f32 v12, v12\nv_exp_f32 v13, v13\nv_exp_f32 v14, v14\nv_exp_f32 v15, v15\n"
"v_exp_f32 v16, v16\nv_exp_f32 v17, v17\nv_exp_f32 v18, v18\nv_exp_f32 v19, v19\ns_waitcnt lgkmcnt(0)\n"
"v_smfmac_f32_32x32x32_bf16 v[32:47], v[64:67], v[8:15], v52\nds_read_b128 v[64:67], v72 offset:3584\nv_add_u32 v72, v72, v73\nv_exp_f32 v20, v20\n"
"v_exp_f32 v21, v21\nv_exp_f32 v22, v22\nv_exp_f32 v23, v23\nv_mfma_f32_32x32x16_bf16 v[0:15], v[56:59], v[68:71], 0\n"
"ds_read_b128 v[56:59], v87 offset:5120\nv_exp_f32 v24, v24\nv_exp_f32 v25, v25\nv_exp_f32 v26, v26\nv_exp_f32 v27, v27\n"
"v_smfmac_f32_32x32x32_bf16 v[32:47], v[60:63], v[16:23], v52\nds_read_b128 v[60:63], v72 offset:0\nv_exp_f32 v28, v28\nv_exp_f32 v29, v29\n"
"v_exp_f32 v30, v30\nv_exp_f32 v31, v31\ns_nop 0\nv_exp_f32 v0, v0\nv_exp_f32 v1, v1\nv_exp_f32 v2, v2\nv_exp_f32 v3, v3\ns_waitcnt lgkmcnt(0)\n"
"v_smfmac_f32_32x32x32_bf16 v[32:47], v[64:67], v[24:31], v52\nds_read_b128 v[64:67], v72 offset:512\ns_setprio 2\nv_exp_f32 v4, v4\nv_exp_f32 v5, v5\n"
"v_exp_f32 v6, v6\nv_exp_f32 v7, v7\nv_mfma_f32_32x32x16_bf16 v[16:31], v[56:59], v[68:71], 0\nds_read_b128 v[56:59], v87 offset:6144\n"
"s_waitcnt vmcnt(4)\nds_write_b128 v74, v[136:139] offset:8192\nv_exp_f32 v8, v8\nv_exp_f32 v9, v9\nv_exp_f32 v10, v10\nv_exp_f32 v11, v11\n"
"v_smfmac_f32_32x32x32_bf16 v[32:47], v[60:63], v[0:7], v52\nds_read_b128 v[60:63], v72 offset:1024\nv_exp_f32 v12, v12\nv_exp_f32 v13, v13\n"
"v_exp_f32 v14, v14\nv_exp_f32 v15, v15\nv_exp_f32 v16, v16\nv_exp_f32 v17, v17\nv_exp_f32 v18, v18\nv_exp_f32 v19, v19\ns_waitcnt lgkmcnt(0)\n"
"v_smfmac_f32_32x32x32_bf16 v[32:47], v[64:67], v[8:15], v52\nds_read_b128 v[64:67], v72 offset:1536\ns_setprio 1\nv_exp_f32 v20, v20\n"
"v_exp_f32 v21, v21\nv_exp_f32 v22, v22\nv_exp_f32 v23, v23\nv_mfma_f32_32x32x16_bf16 v[0:15], v[56:59], v[68:71], 0\n"
"ds_read_b128 v[56:59], v87 offset:7168\ns_waitcnt vmcnt(3)\nds_write_b128 v75, v[140:143] offset:8192\nv_exp_f32 v24, v24\nv_exp_f32 v25, v25\n"
"v_exp_f32 v26, v26\nv_exp_f32 v27, v27\nv_smfmac_f32_32x32x32_bf16 v[32:47], v[60:63], v[16:23], v52\nds_read_b128 v[60:63], v72 offset:2048\n"
"s_setprio 0\nv_exp_f32 v28, v28\nv_exp_f32 v29, v29\nv_exp_f32 v30, v30\nv_exp_f32 v31, v31\nv_exp_f32 v0, v0\nv_exp_f32 v1, v1\nv_exp_f32 v2, v2\n"
"v_exp_f32 v3, v3\ns_waitcnt vmcnt(0) lgkmcnt(0)\ns_barrier\nv_smfmac_f32_32x32x32_bf16 v[32:47], v[64:67], v[24:31], v52\n"
"ds_read_b128 v[64:67], v72 offset:2560\ns_setprio 3\nv_cmp_eq_u64 vcc, s[40:41], v[76:77]\ns_nop 0\ns_mov_b32 s100, vcc_lo\ns_lshr_b32 s47, s100, 8\n"
"s_and_b32 s100, s100, s47\nv_mov_b32 v84, s40\nv_mov_b32 v85, s41\ns_cmp_lg_u32 s42, 0\ns_cbranch_scc1 .Lfl\ns_mov_b64 exec, 1\n"
"buffer_store_dwordx2 v[84:85], off, s[12:15], s50 sc1\ns_mov_b64 exec, -1\n.Lfl:\nv_exp_f32 v4, v4\nv_exp_f32 v5, v5\nv_exp_f32 v6, v6\n"
"v_exp_f32 v7, v7\nv_mfma_f32_32x32x16_bf16 v[16:31], v[56:59], v[68:71], 0\nds_read_b128 v[56:59], v87 offset:8192\ns_bitcmp1_b32 s100, s3\n"
"s_cbranch_scc0 .Lsl2\n.Lsr2:\ns_lshl_b32 s47, s3, 13\ns_add_u32 s3, s3, 2\ns_add_u32 s47, s47, s49\ns_add_u32 s53, s47, 0x200000\n"
"buffer_load_dwordx4 v[144:147], v87, s[12:15], s47 offen sc1\nbuffer_load_dwordx4 v[148:151], v87, s[12:15], s53 offen sc1\nv_exp_f32 v8, v8\n"
"v_exp_f32 v9, v9\nv_exp_f32 v10, v10\nv_exp_f32 v11, v11\nv_smfmac_f32_32x32x32_bf16 v[32:47], v[60:63], v[0:7], v52\n"
"ds_read_b128 v[60:63], v72 offset:3072\nds_read_b64_tr_b16 v[88:89], v102 offset:0\nds_read_b64_tr_b16 v[90:91], v102 offset:512\nv_exp_f32 v12, v12\n"
"v_exp_f32 v13, v13\nv_exp_f32 v14, v14\nv_exp_f32 v15, v15\nv_exp_f32 v16, v16\nv_exp_f32 v17, v17\nv_exp_f32 v18, v18\nv_exp_f32 v19, v19\n"
"s_waitcnt lgkmcnt(0)\nv_smfmac_f32_32x32x32_bf16 v[32:47], v[64:67], v[8:15], v52\nds_read_b128 v[64:67], v72 offset:3584\nv_add_u32 v72, v72, v73\n"
"v_exp_f32 v20, v20\nv_exp_f32 v21, v21\nv_exp_f32 v22, v22\nv_exp_f32 v23, v23\nv_mfma_f32_32x32x16_bf16 v[0:15], v[56:59], v[68:71], 0\n"
"ds_read_b128 v[56:59], v87 offset:9216\ns_bitcmp1_b32 s100, s3\ns_cbranch_scc0 .Lsl3\n.Lsr3:\ns_lshl_b32 s47, s3, 13\ns_xor_b32 s3, s3, 7\n"
"s_add_u32 s47, s47, s49\ns_add_u32 s53, s47, 0x200000\nbuffer_load_dwordx4 v[152:155], v87, s[12:15], s47 offen sc1\n"
"buffer_load_dwordx4 v[156:159], v87, s[12:15], s53 offen sc1\nv_exp_f32 v24, v24\nv_exp_f32 v25, v25\nv_exp_f32 v26, v26\nv_exp_f32 v27, v27\n"
"v_smfmac_f32_32x32x32_bf16 v[32:47], v[60:63], v[16:23], v52\nds_read_b128 v[60:63], v72 offset:0\nds_read_b64_tr_b16 v[92:93], v102 offset:1024\n"
"ds_read_b64_tr_b16 v[94:95], v102 offset:1536\nv_exp_f32 v28, v28\nv_exp_f32 v29, v29\nv_exp_f32 v30, v30\nv_exp_f32 v31, v31\nv_exp_f32 v0, v0\n"
"v_exp_f32 v1, v1\nv_exp_f32 v2, v2\nv_exp_f32 v3, v3\ns_waitcnt lgkmcnt(0)\nv_smfmac_f32_32x32x32_bf16 v[32:47], v[64:67], v[24:31], v52\n"
"ds_read_b128 v[64:67], v72 offset:512\nv_exp_f32 v4, v4\nv_exp_f32 v5, v5\nv_exp_f32 v6, v6\nv_exp_f32 v7, v7\n"
"v_mfma_f32_32x32x16_bf16 v[16:31], v[56:59], v[68:71], 0\nds_read_b128 v[56:59], v87 offset:10240\n"
"buffer_store_dwordx4 v[88:91], v87, s[12:15], s54 offen sc1\nv_exp_f32 v8, v8\nv_exp_f32 v9, v9\nv_exp_f32 v10, v10\nv_exp_f32 v11, v11\n"
"v_smfmac_f32_32x32x32_bf16 v[32:47], v[60:63], v[0:7], v52\nds_read_b128 v[60:63], v72 offset:1024\nv_exp_f32 v12, v12\nv_exp_f32 v13, v13\n"
"v_exp_f32 v14, v14\nv_exp_f32 v15, v15\nv_exp_f32 v16, v16\nv_exp_f32 v17, v17\nv_exp_f32 v18, v18\nv_exp_f32 v19, v19\ns_waitcnt lgkmcnt(0)\n"
"v_smfmac_f32_32x32x32_bf16 v[32:47], v[64:67], v[8:15], v52\nds_read_b128 v[64:67], v72 offset:1536\nv_exp_f32 v20, v20\nv_exp_f32 v21, v21\n"
"v_exp_f32 v22, v22\nv_exp_f32 v23, v23\nv_mfma_f32_32x32x16_bf16 v[0:15], v[56:59], v[68:71], 0\nds_read_b128 v[56:59], v87 offset:11264\n"
"buffer_store_dwordx4 v[92:95], v87, s[12:15], s55 offen sc1\nv_exp_f32 v24, v24\nv_exp_f32 v25, v25\nv_exp_f32 v26, v26\nv_exp_f32 v27, v27\n"
"v_smfmac_f32_32x32x32_bf16 v[32:47], v[60:63], v[16:23], v52\nds_read_b128 v[60:63], v72 offset:2048\nv_exp_f32 v28, v28\nv_exp_f32 v29, v29\n"
"v_exp_f32 v30, v30\nv_exp_f32 v31, v31\nv_exp_f32 v0, v0\nv_exp_f32 v1, v1\nv_exp_f32 v2, v2\nv_exp_f32 v3, v3\ns_waitcnt lgkmcnt(0)\n"
"v_smfmac_f32_32x32x32_bf16 v[32:47], v[64:67], v[24:31], v52\nds_read_b128 v[64:67], v72 offset:2560\ns_waitcnt vmcnt(5)\n"
"ds_write_b128 v74, v[144:147] offset:16384\nv_exp_f32 v4, v4\nv_exp_f32 v5, v5\nv_exp_f32 v6, v6\nv_exp_f32 v7, v7\n"
"v_mfma_f32_32x32x16_bf16 v[16:31], v[56:59], v[68:71], 0\nds_read_b128 v[56:59], v87 offset:12288\nv_exp_f32 v8, v8\nv_exp_f32 v9, v9\n"
"v_exp_f32 v10, v10\nv_exp_f32 v11, v11\nv_smfmac_f32_32x32x32_bf16 v[32:47], v[60:63], v[0:7], v52\nds_read_b128 v[60:63], v72 offset:3072\n"
"v_exp_f32 v12, v12\nv_exp_f32 v13, v13\nv_exp_f32 v14, v14\nv_exp_f32 v15, v15\ns_nop 0\nv_exp_f32 v16, v16\nv_exp_f32 v17, v17\nv_exp_f32 v18, v18\n"
"v_exp_f32 v19, v19\ns_waitcnt lgkmcnt(0)\nv_smfmac_f32_32x32x32_bf16 v[32:47], v[64:67], v[8:15], v52\nds_read_b128 v[64:67], v72 offset:3584\n"
"s_waitcnt vmcnt(4)\nds_write_b128 v75, v[148:151] offset:16384\nv_add_u32 v72, v72, v73\nv_exp_f32 v20, v20\nv_exp_f32 v21, v21\nv_exp_f32 v22, v22\n"
"v_exp_f32 v23, v23\nv_mfma_f32_32x32x16_bf16 v[0:15], v[56:59], v[68:71], 0\nds_read_b128 v[56:59], v87 offset:13312\nv_exp_f32 v24, v24\n"
"v_exp_f32 v25, v25\nv_exp_f32 v26, v26\nv_exp_f32 v27, v27\nv_smfmac_f32_32x32x32_bf16 v[32:47], v[60:63], v[16:23], v52\n"
"ds_read_b128 v[60:63], v72 offset:0\nv_exp_f32 v28, v28\nv_exp_f32 v29, v29\nv_exp_f32 v30, v30\nv_exp_f32 v31, v31\ns_nop 0\nv_exp_f32 v0, v0\n"
"v_exp_f32 v1, v1\nv_exp_f32 v2, v2\nv_exp_f32 v3, v3\ns_waitcnt lgkmcnt(0)\nv_smfmac_f32_32x32x32_bf16 v[32:47], v[64:67], v[24:31], v52\n"
"ds_read_b128 v[64:67], v72 offset:512\ns_setprio 2\ns_waitcnt vmcnt(3)\nds_write_b128 v74, v[152:155] offset:24576\nv_exp_f32 v4, v4\n"
"v_exp_f32 v5, v5\nv_exp_f32 v6, v6\nv_exp_f32 v7, v7\nv_mfma_f32_32x32x16_bf16 v[16:31], v[56:59], v[68:71], 0\n"
"ds_read_b128 v[56:59], v87 offset:14336\nv_exp_f32 v8, v8\nv_exp_f32 v9, v9\nv_exp_f32 v10, v10\nv_exp_f32 v11, v11\n"
"v_smfmac_f32_32x32x32_bf16 v[32:47], v[60:63], v[0:7], v52\nds_read_b128 v[60:63], v72 offset:1024\nv_exp_f32 v12, v12\nv_exp_f32 v13, v13\n"
"v_exp_f32 v14, v14\nv_exp_f32 v15, v15\ns_nop 0\nv_exp_f32 v16, v16\nv_exp_f32 v17, v17\nv_exp_f32 v18, v18\nv_exp_f32 v19, v19\n"
"s_waitcnt lgkmcnt(0)\nv_smfmac_f32_32x32x32_bf16 v[32:47], v[64:67], v[8:15], v52\nds_read_b128 v[64:67], v72 offset:1536\ns_setprio 1\n"
"s_waitcnt vmcnt(2)\nds_write_b128 v75, v[156:159] offset:24576\nv_exp_f32 v20, v20\nv_exp_f32 v21, v21\nv_exp_f32 v22, v22\nv_exp_f32 v23, v23\n"
"v_mfma_f32_32x32x16_bf16 v[0:15], v[56:59], v[68:71], 0\nds_read_b128 v[56:59], v87 offset:15360\nv_exp_f32 v24, v24\nv_exp_f32 v25, v25\n"
"v_exp_f32 v26, v26\nv_exp_f32 v27, v27\nv_smfmac_f32_32x32x32_bf16 v[32:47], v[60:63], v[16:23], v52\nds_read_b128 v[60:63], v72 offset:2048\n"
"s_setprio 0\nv_exp_f32 v28, v28\nv_exp_f32 v29, v29\nv_exp_f32 v30, v30\nv_exp_f32 v31, v31\nv_exp_f32 v0, v0\nv_exp_f32 v1, v1\nv_exp_f32 v2, v2\n"
"v_exp_f32 v3, v3\ns_waitcnt vmcnt(0) lgkmcnt(0)\ns_barrier\nv_smfmac_f32_32x32x32_bf16 v[32:47], v[64:67], v[24:31], v52\n"
"ds_read_b128 v[64:67], v72 offset:2560\ns_setprio 3\nv_mov_b32 v84, s40\nv_mov_b32 v85, s41\ns_cmp_lg_u32 s42, 0\ns_cbranch_scc1 .Lfw\n"
"s_mov_b64 exec, 1\nbuffer_store_dwordx2 v[84:85], off, s[12:15], s51 sc1\ns_mov_b64 exec, -1\n.Lfw:\nv_exp_f32 v4, v4\nv_exp_f32 v5, v5\n"
"v_exp_f32 v6, v6\nv_exp_f32 v7, v7\nv_mfma_f32_32x32x16_bf16 v[16:31], v[56:59], v[68:71], 0\nds_read_b128 v[56:59], v87 offset:16384\n"
"v_exp_f32 v8, v8\nv_exp_f32 v9, v9\nv_exp_f32 v10, v10\nv_exp_f32 v11, v11\nv_smfmac_f32_32x32x32_bf16 v[32:47], v[60:63], v[0:7], v52\n"
"ds_read_b128 v[60:63], v72 offset:3072\nbuffer_load_dwordx2 v[76:77], v78, s[12:15], 0 offen sc1\nv_exp_f32 v12, v12\nv_exp_f32 v13, v13\n"
"v_exp_f32 v14, v14\nv_exp_f32 v15, v15\nv_exp_f32 v16, v16\nv_exp_f32 v17, v17\nv_exp_f32 v18, v18\nv_exp_f32 v19, v19\ns_waitcnt lgkmcnt(0)\n"
"v_smfmac_f32_32x32x32_bf16 v[32:47], v[64:67], v[8:15], v52\nds_read_b128 v[64:67], v72 offset:3584\nv_add_u32 v72, v72, v73\nv_exp_f32 v20, v20\n"
"v_exp_f32 v21, v21\nv_exp_f32 v22, v22\nv_exp_f32 v23, v23\nv_mfma_f32_32x32x16_bf16 v[0:15], v[56:59], v[68:71], 0\n"
"ds_read_b128 v[56:59], v87 offset:17408\nv_exp_f32 v24, v24\nv_exp_f32 v25, v25\nv_exp_f32 v26, v26\nv_exp_f32 v27, v27\n"
"v_smfmac_f32_32x32x32_bf16 v[32:47], v[60:63], v[16:23], v52\nds_read_b128 v[60:63], v72 offset:0\nv_exp_f32 v28, v28\nv_exp_f32 v29, v29\n"
"v_exp_f32 v30, v30\nv_exp_f32 v31, v31\ns_nop 0\nv_exp_f32 v0, v0\nv_exp_f32 v1, v1\nv_exp_f32 v2, v2\nv_exp_f32 v3, v3\ns_waitcnt lgkmcnt(0)\n"
"v_smfmac_f32_32x32x32_bf16 v[32:47], v[64:67], v[24:31], v52\nds_read_b128 v[64:67], v72 offset:512\nv_exp_f32 v4, v4\nv_exp_f32 v5, v5\n"
"v_exp_f32 v6, v6\nv_exp_f32 v7, v7\nv_mfma_f32_32x32x16_bf16 v[16:31], v[56:59], v[68:71], 0\nds_read_b128 v[56:59], v87 offset:18432\n"
"v_exp_f32 v8, v8\nv_exp_f32 v9, v9\nv_exp_f32 v10, v10\nv_exp_f32 v11, v11\nv_smfmac_f32_32x32x32_bf16 v[32:47], v[60:63], v[0:7], v52\n"
"ds_read_b128 v[60:63], v72 offset:1024\nv_exp_f32 v12, v12\nv_exp_f32 v13, v13\nv_exp_f32 v14, v14\nv_exp_f32 v15, v15\ns_nop 0\nv_exp_f32 v16, v16\n"
"v_exp_f32 v17, v17\nv_exp_f32 v18, v18\nv_exp_f32 v19, v19\ns_waitcnt lgkmcnt(0)\nv_smfmac_f32_32x32x32_bf16 v[32:47], v[64:67], v[8:15], v52\n"
"ds_read_b128 v[64:67], v72 offset:1536\nv_exp_f32 v20, v20\nv_exp_f32 v21, v21\nv_exp_f32 v22, v22\nv_exp_f32 v23, v23\n"
"v_mfma_f32_32x32x16_bf16 v[0:15], v[56:59], v[68:71], 0\nds_read_b128 v[56:59], v87 offset:19456\ns_waitcnt vmcnt(0)\n"
"v_cmp_eq_u64 vcc, s[40:41], v[76:77]\ns_nop 0\ns_mov_b32 s100, vcc_lo\ns_lshr_b32 s47, s100, 8\ns_and_b32 s100, s100, s47\ns_bitcmp1_b32 s100, s3\n"
"s_cbranch_scc0 .Lsl4\n.Lsr4:\ns_lshl_b32 s47, s3, 13\ns_add_u32 s3, s3, 2\ns_add_u32 s47, s47, s49\ns_add_u32 s53, s47, 0x200000\n"
"buffer_load_dwordx4 v[160:163], v87, s[12:15], s47 offen sc1\nbuffer_load_dwordx4 v[164:167], v87, s[12:15], s53 offen sc1\nv_exp_f32 v24, v24\n"
"v_exp_f32 v25, v25\nv_exp_f32 v26, v26\nv_exp_f32 v27, v27\nv_smfmac_f32_32x32x32_bf16 v[32:47], v[60:63], v[16:23], v52\n"
"ds_read_b128 v[60:63], v72 offset:2048\nbuffer_load_dwordx2 v[76:77], v78, s[12:15], 0 offen sc1\nv_exp_f32 v28, v28\nv_exp_f32 v29, v29\n"
"v_exp_f32 v30, v30\nv_exp_f32 v31, v31\nv_exp_f32 v0, v0\nv_exp_f32 v1, v1\nv_exp_f32 v2, v2\nv_exp_f32 v3, v3\ns_waitcnt lgkmcnt(0)\n"
"v_smfmac_f32_32x32x32_bf16 v[32:47], v[64:67], v[24:31], v52\nds_read_b128 v[64:67], v72 offset:2560\nv_exp_f32 v4, v4\nv_exp_f32 v5, v5\n"
"v_exp_f32 v6, v6\nv_exp_f32 v7, v7\nv_mfma_f32_32x32x16_bf16 v[16:31], v[56:59], v[68:71], 0\nds_read_b128 v[56:59], v87 offset:20480\n"
"s_bitcmp1_b32 s100, s3\ns_cbranch_scc0 .Lsl5\n.Lsr5:\ns_lshl_b32 s47, s3, 13\ns_add_u32 s3, s3, 2\ns_add_u32 s47, s47, s49\n"
"s_add_u32 s53, s47, 0x200000\nbuffer_load_dwordx4 v[168:171], v87, s[12:15], s47 offen sc1\n"
"buffer_load_dwordx4 v[172:175], v87, s[12:15], s53 offen sc1\nv_exp_f32 v8, v8\nv_exp_f32 v9, v9\nv_exp_f32 v10, v10\nv_exp_f32 v11, v11\n"
"v_smfmac_f32_32x32x32_bf16 v[32:47], v[60:63], v[0:7], v52\nds_read_b128 v[60:63], v72 offset:3072\nv_exp_f32 v12, v12\nv_exp_f32 v13, v13\n"
"v_exp_f32 v14, v14\nv_exp_f32 v15, v15\nv_exp_f32 v16, v16\nv_exp_f32 v17, v17\nv_exp_f32 v18, v18\nv_exp_f32 v19, v19\ns_waitcnt lgkmcnt(0)\n"
"v_smfmac_f32_32x32x32_bf16 v[32:47], v[64:67], v[8:15], v52\nds_read_b128 v[64:67], v72 offset:3584\nv_add_u32 v72, v72, v73\nv_exp_f32 v20, v20\n"
"v_exp_f32 v21, v21\nv_exp_f32 v22, v22\nv_exp_f32 v23, v23\nv_mfma_f32_32x32x16_bf16 v[0:15], v[56:59], v[68:71], 0\n"
"ds_read_b128 v[56:59], v87 offset:21504\nv_exp_f32 v24, v24\nv_exp_f32 v25, v25\nv_exp_f32 v26, v26\nv_exp_f32 v27, v27\n"
"v_smfmac_f32_32x32x32_bf16 v[32:47], v[60:63], v[16:23], v52\nds_read_b128 v[60:63], v72 offset:0\nv_exp_f32 v28, v28\nv_exp_f32 v29, v29\n"
"v_exp_f32 v30, v30\nv_exp_f32 v31, v31\ns_nop 0\nv_exp_f32 v0, v0\nv_exp_f32 v1, v1\nv_exp_f32 v2, v2\nv_exp_f32 v3, v3\ns_waitcnt lgkmcnt(0)\n"
"v_smfmac_f32_32x32x32_bf16 v[32:47], v[64:67], v[24:31], v52\nds_read_b128 v[64:67], v72 offset:512\nv_exp_f32 v4, v4\nv_exp_f32 v5, v5\n"
"v_exp_f32 v6, v6\nv_exp_f32 v7, v7\nv_mfma_f32_32x32x16_bf16 v[16:31], v[56:59], v[68:71], 0\nds_read_b128 v[56:59], v87 offset:22528\n"
"v_exp_f32 v8, v8\nv_exp_f32 v9, v9\nv_exp_f32 v10, v10\nv_exp_f32 v11, v11\nv_smfmac_f32_32x32x32_bf16 v[32:47], v[60:63], v[0:7], v52\n"
"ds_read_b128 v[60:63], v72 offset:1024\nbuffer_load_dwordx2 v[84:85], v86, s[12:15], 0 offen sc1\nv_exp_f32 v12, v12\nv_exp_f32 v13, v13\n"
"v_exp_f32 v14, v14\nv_exp_f32 v15, v15\nv_exp_f32 v16, v16\nv_exp_f32 v17, v17\nv_exp_f32 v18, v18\nv_exp_f32 v19, v19\ns_waitcnt lgkmcnt(0)\n"
"v_smfmac_f32_32x32x32_bf16 v[32:47], v[64:67], v[8:15], v52\nds_read_b128 v[64:67], v72 offset:1536\nv_exp_f32 v20, v20\nv_exp_f32 v21, v21\n"
"v_exp_f32 v22, v22\nv_exp_f32 v23, v23\nv_mfma_f32_32x32x16_bf16 v[0:15], v[56:59], v[68:71], 0\nds_read_b128 v[56:59], v87 offset:23552\n"
"s_waitcnt vmcnt(3)\nv_cmp_eq_u64 vcc, s[40:41], v[76:77]\ns_nop 0\ns_mov_b32 s100, vcc_lo\ns_lshr_b32 s47, s100, 8\ns_and_b32 s100, s100, s47\n"
"s_bitcmp1_b32 s100, s3\ns_cbranch_scc0 .Lsl6\n.Lsr6:\ns_lshl_b32 s47, s3, 13\ns_add_u32 s3, s3, 2\ns_add_u32 s47, s47, s49\n"
"s_add_u32 s53, s47, 0x200000\nbuffer_load_dwordx4 v[176:179], v87, s[12:15], s47 offen sc1\n"
"buffer_load_dwordx4 v[180:183], v87, s[12:15], s53 offen sc1\nv_exp_f32 v24, v24\nv_exp_f32 v25, v25\nv_exp_f32 v26, v26\nv_exp_f32 v27, v27\n"
"v_smfmac_f32_32x32x32_bf16 v[32:47], v[60:63], v[16:23], v52\nds_read_b128 v[60:63], v72 offset:2048\nv_exp_f32 v28, v28\nv_exp_f32 v29, v29\n"
"v_exp_f32 v30, v30\nv_exp_f32 v31, v31\nv_exp_f32 v0, v0\nv_exp_f32 v1, v1\nv_exp_f32 v2, v2\nv_exp_f32 v3, v3\ns_waitcnt lgkmcnt(0)\n"
"v_smfmac_f32_32x32x32_bf16 v[32:47], v[64:67], v[24:31], v52\nds_read_b128 v[64:67], v72 offset:2560\nds_write_b128 v74, v[160:163] offset:32768\n"
"v_exp_f32 v4, v4\nv_exp_f32 v5, v5\nv_exp_f32 v6, v6\nv_exp_f32 v7, v7\nv_mfma_f32_32x32x16_bf16 v[16:31], v[56:59], v[68:71], 0\n"
"ds_read_b128 v[56:59], v87 offset:24576\ns_bitcmp1_b32 s100, s3\ns_cbranch_scc0 .Lsl7\n.Lsr7:\ns_lshl_b32 s47, s3, 13\ns_add_u32 s3, s3, 2\n"
"s_add_u32 s47, s47, s49\ns_add_u32 s53, s47, 0x200000\nbuffer_load_dwordx4 v[184:187], v87, s[12:15], s47 offen sc1\n"
"buffer_load_dwordx4 v[188:191], v87, s[12:15], s53 offen sc1\nv_exp_f32 v8, v8\nv_exp_f32 v9, v9\nv_exp_f32 v10, v10\nv_exp_f32 v11, v11\n"
"v_smfmac_f32_32x32x32_bf16 v[32:47], v[60:63], v[0:7], v52\nds_read_b128 v[60:63], v72 offset:3072\nv_exp_f32 v12, v12\nv_exp_f32 v13, v13\n"
"v_exp_f32 v14, v14\nv_exp_f32 v15, v15\nv_exp_f32 v16, v16\nv_exp_f32 v17, v17\nv_exp_f32 v18, v18\nv_exp_f32 v19, v19\ns_waitcnt lgkmcnt(0)\n"
"v_smfmac_f32_32x32x32_bf16 v[32:47], v[64:67], v[8:15], v52\nds_read_b128 v[64:67], v72 offset:3584\nds_write_b128 v75, v[164:167] offset:32768\n"
"v_add_u32 v72, v72, v73\nv_exp_f32 v20, v20\nv_exp_f32 v21, v21\nv_exp_f32 v22, v22\nv_exp_f32 v23, v23\n"
"v_mfma_f32_32x32x16_bf16 v[0:15], v[56:59], v[68:71], 0\nds_read_b128 v[56:59], v87 offset:25600\nv_exp_f32 v24, v24\nv_exp_f32 v25, v25\n"
"v_exp_f32 v26, v26\nv_exp_f32 v27, v27\nv_smfmac_f32_32x32x32_bf16 v[32:47], v[60:63], v[16:23], v52\nds_read_b128 v[60:63], v72 offset:0\n"
"v_exp_f32 v28, v28\nv_exp_f32 v29, v29\nv_exp_f32 v30, v30\nv_exp_f32 v31, v31\ns_nop 0\nv_exp_f32 v0, v0\nv_exp_f32 v1, v1\nv_exp_f32 v2, v2\n"
"v_exp_f32 v3, v3\ns_waitcnt lgkmcnt(0)\nv_smfmac_f32_32x32x32_bf16 v[32:47], v[64:67], v[24:31], v52\nds_read_b128 v[64:67], v72 offset:512\n"
"s_waitcnt vmcnt(6)\nds_write_b128 v74, v[168:171] offset:40960\nv_exp_f32 v4, v4\nv_exp_f32 v5, v5\nv_exp_f32 v6, v6\nv_exp_f32 v7, v7\n"
"v_mfma_f32_32x32x16_bf16 v[16:31], v[56:59], v[68:71], 0\nds_read_b128 v[56:59], v87 offset:26624\nv_exp_f32 v8, v8\nv_exp_f32 v9, v9\n"
"v_exp_f32 v10, v10\nv_exp_f32 v11, v11\nv_smfmac_f32_32x32x32_bf16 v[32:47], v[60:63], v[0:7], v52\nds_read_b128 v[60:63], v72 offset:1024\n"
"s_waitcnt vmcnt(4)\nv_cmp_ne_u64 vcc, s[40:41], v[84:85]\ns_nop 0\ns_cbranch_vccz .Lpw_ok\ns_mov_b32 s47, 0\ns_nop 5\n.Lpw:\n"
"buffer_load_dwordx2 v[84:85], v86, s[12:15], 0 offen sc1\ns_waitcnt vmcnt(0)\nv_cmp_ne_u64 vcc, s[40:41], v[84:85]\ns_add_u32 s47, s47, 1\n"
"s_cmp_lt_u32 s47, 0x40000\ns_cselect_b64 vcc, vcc, 0\ns_cbranch_vccnz .Lpw\n.Lpw_ok:\nv_exp_f32 v12, v12\nv_exp_f32 v13, v13\nv_exp_f32 v14, v14\n"
"v_exp_f32 v15, v15\ns_and_b32 s47, s2, 0xf1\ns_xor_b32 s48, s42, 7\ns_or_b32 s47, s47, s48\ns_cmp_lg_u32 s47, 0\ns_cbranch_scc1 .Lpq0\n"
"s_lshl_b32 s28, s2, 20\ns_add_u32 s28, s28, 0x1d00000\ns_add_u32 s29, s28, 0x2000\nv_not_b32 v76, s40\nv_not_b32 v77, s40\ns_mov_b64 exec, 1\n"
"buffer_store_dwordx2 v[76:77], off, s[12:15], s28 offset:0 sc1\nbuffer_store_dwordx2 v[76:77], off, s[12:15], s29 offset:0 sc1\n"
"buffer_store_dwordx2 v[76:77], off, s[12:15], s28 offset:128 sc1\nbuffer_store_dwordx2 v[76:77], off, s[12:15], s29 offset:128 sc1\n"
"s_mov_b64 exec, -1\n.Lpq0:\nv_exp_f32 v16, v16\nv_exp_f32 v17, v17\nv_exp_f32 v18, v18\nv_exp_f32 v19, v19\ns_waitcnt lgkmcnt(0)\n"
"v_smfmac_f32_32x32x32_bf16 v[32:47], v[64:67], v[8:15], v52\nds_read_b128 v[64:67], v72 offset:1536\nds_write_b128 v75, v[172:175] offset:40960\n"
"v_exp_f32 v20, v20\nv_exp_f32 v21, v21\nv_exp_f32 v22, v22\nv_exp_f32 v23, v23\nv_mfma_f32_32x32x16_bf16 v[0:15], v[56:59], v[68:71], 0\n"
"ds_read_b128 v[56:59], v87 offset:27648\ns_add_u32 s101, s58, 0x4000\nbuffer_load_dwordx4 v[192:195], v79, s[12:15], s101 offen offset:0 sc1\n"
"v_exp_f32 v24, v24\nv_exp_f32 v25, v25\nv_exp_f32 v26, v26\nv_exp_f32 v27, v27\nv_smfmac_f32_32x32x32_bf16 v[32:47], v[60:63], v[16:23], v52\n"
"ds_read_b128 v[60:63], v72 offset:2048\ns_add_u32 s101, s60, 0x0\nbuffer_load_dwordx4 v[88:91], v87, s[12:15], s101 offen offset:0 sc1\n"
"v_exp_f32 v28, v28\nv_exp_f32 v29, v29\nv_exp_f32 v30, v30\nv_exp_f32 v31, v31\nv_exp_f32 v0, v0\nv_exp_f32 v1, v1\nv_exp_f32 v2, v2\n"
"v_exp_f32 v3, v3\ns_waitcnt lgkmcnt(0)\nv_smfmac_f32_32x32x32_bf16 v[32:47], v[64:67], v[24:31], v52\nds_read_b128 v[64:67], v72 offset:2560\n"
"v_exp_f32 v4, v4\nv_exp_f32 v5, v5\nv_exp_f32 v6, v6\nv_exp_f32 v7, v7\nv_mfma_f32_32x32x16_bf16 v[16:31], v[56:59], v[68:71], 0\n"
"ds_read_b128 v[56:59], v87 offset:28672\ns_add_u32 s101, s58, 0x4000\nbuffer_load_dwordx4 v[196:199], v79, s[12:15], s101 offen offset:1024 sc1\n"
"v_exp_f32 v8, v8\nv_exp_f32 v9, v9\nv_exp_f32 v10, v10\nv_exp_f32 v11, v11\nv_smfmac_f32_32x32x32_bf16 v[32:47], v[60:63], v[0:7], v52\n"
"ds_read_b128 v[60:63], v72 offset:3072\ns_add_u32 s101, s60, 0x0\nbuffer_load_dwordx4 v[92:95], v87, s[12:15], s101 offen offset:1024 sc1\n"
"v_exp_f32 v12, v12\nv_exp_f32 v13, v13\nv_exp_f32 v14, v14\nv_exp_f32 v15, v15\nv_exp_f32 v16, v16\nv_exp_f32 v17, v17\nv_exp_f32 v18, v18\n"
"v_exp_f32 v19, v19\ns_waitcnt lgkmcnt(0)\nv_smfmac_f32_32x32x32_bf16 v[32:47], v[64:67], v[8:15], v52\nds_read_b128 v[64:67], v72 offset:3584\n"
"s_setprio 2\ns_waitcnt vmcnt(7)\nds_write_b128 v74, v[176:179] offset:49152\nv_add_u32 v72, v72, v73\nv_exp_f32 v20, v20\nv_exp_f32 v21, v21\n"
"v_exp_f32 v22, v22\nv_exp_f32 v23, v23\nv_mfma_f32_32x32x16_bf16 v[0:15], v[56:59], v[68:71], 0\nds_read_b128 v[56:59], v87 offset:29696\n"
"s_add_u32 s101, s58, 0x4000\nbuffer_load_dwordx4 v[200:203], v79, s[12:15], s101 offen offset:2048 sc1\nv_exp_f32 v24, v24\nv_exp_f32 v25, v25\n"
"v_exp_f32 v26, v26\nv_exp_f32 v27, v27\nv_smfmac_f32_32x32x32_bf16 v[32:47], v[60:63], v[16:23], v52\nds_read_b128 v[60:63], v72 offset:0\n"
"s_add_u32 s101, s60, 0x0\nbuffer_load_dwordx4 v[96:99], v87, s[12:15], s101 offen offset:2048 sc1\nv_exp_f32 v28, v28\nv_exp_f32 v29, v29\n"
"v_exp_f32 v30, v30\nv_exp_f32 v31, v31\nv_exp_f32 v0, v0\nv_exp_f32 v1, v1\nv_exp_f32 v2, v2\nv_exp_f32 v3, v3\ns_waitcnt lgkmcnt(0)\n"
"v_smfmac_f32_32x32x32_bf16 v[32:47], v[64:67], v[24:31], v52\nds_read_b128 v[64:67], v72 offset:512\ns_waitcnt vmcnt(8)\n"
"ds_write_b128 v75, v[180:183] offset:49152\nv_exp_f32 v4, v4\nv_exp_f32 v5, v5\nv_exp_f32 v6, v6\nv_exp_f32 v7, v7\n"
"v_mfma_f32_32x32x16_bf16 v[16:31], v[56:59], v[68:71], 0\nds_read_b128 v[56:59], v87 offset:30720\ns_add_u32 s101, s58, 0x4000\n"
"buffer_load_dwordx4 v[204:207], v79, s[12:15], s101 offen offset:3072 sc1\nv_exp_f32 v8, v8\nv_exp_f32 v9, v9\nv_exp_f32 v10, v10\n"
"v_exp_f32 v11, v11\nv_smfmac_f32_32x32x32_bf16 v[32:47], v[60:63], v[0:7], v52\nds_read_b128 v[60:63], v72 offset:1024\ns_add_u32 s101, s60, 0x0\n"
"buffer_load_dwordx4 v[100:103], v87, s[12:15], s101 offen offset:3072 sc1\nv_exp_f32 v12, v12\nv_exp_f32 v13, v13\nv_exp_f32 v14, v14\n"
"v_exp_f32 v15, v15\nv_exp_f32 v16, v16\nv_exp_f32 v17, v17\nv_exp_f32 v18, v18\nv_exp_f32 v19, v19\ns_waitcnt lgkmcnt(0)\n"
"v_smfmac_f32_32x32x32_bf16 v[32:47], v[64:67], v[8:15], v52\nds_read_b128 v[64:67], v72 offset:1536\ns_waitcnt vmcnt(9)\n"
"ds_write_b128 v74, v[184:187] offset:57344\nv_exp_f32 v20, v20\nv_exp_f32 v21, v21\nv_exp_f32 v22, v22\nv_exp_f32 v23, v23\n"
"v_mfma_f32_32x32x16_bf16 v[0:15], v[56:59], v[68:71], 0\nds_read_b128 v[56:59], v87 offset:31744\ns_add_u32 s101, s58, 0x5000\n"
"buffer_load_dwordx4 v[208:211], v79, s[12:15], s101 offen offset:0 sc1\nv_exp_f32 v24, v24\nv_exp_f32 v25, v25\nv_exp_f32 v26, v26\n"
"v_exp_f32 v27, v27\nv_smfmac_f32_32x32x32_bf16 v[32:47], v[60:63], v[16:23], v52\nds_read_b128 v[60:63], v72 offset:2048\n"
"s_add_u32 s101, s60, 0x1000\nbuffer_load_dwordx4 v[104:107], v87, s[12:15], s101 offen offset:0 sc1\nv_exp_f32 v28, v28\nv_exp_f32 v29, v29\n"
"v_exp_f32 v30, v30\nv_exp_f32 v31, v31\nv_exp_f32 v0, v0\nv_exp_f32 v1, v1\nv_exp_f32 v2, v2\nv_exp_f32 v3, v3\ns_waitcnt lgkmcnt(0)\n"
"v_smfmac_f32_32x32x32_bf16 v[32:47], v[64:67], v[24:31], v52\nds_read_b128 v[64:67], v72 offset:2560\nds_read_b128 v[80:83], v72 offset:3584\n"
"s_setprio 1\ns_waitcnt vmcnt(10)\nds_write_b128 v75, v[188:191] offset:57344\nv_exp_f32 v4, v4\nv_exp_f32 v5, v5\nv_exp_f32 v6, v6\nv_exp_f32 v7, v7\n"
"v_mfma_f32_32x32x16_bf16 v[16:31], v[56:59], v[68:71], 0\ns_add_u32 s101, s58, 0x5000\n"
"buffer_load_dwordx4 v[212:215], v79, s[12:15], s101 offen offset:1024 sc1\nv_exp_f32 v8, v8\nv_exp_f32 v9, v9\nv_exp_f32 v10, v10\n"
"v_exp_f32 v11, v11\nv_smfmac_f32_32x32x32_bf16 v[32:47], v[60:63], v[0:7], v52\nds_read_b128 v[60:63], v72 offset:3072\ns_setprio 0\n"
"s_add_u32 s101, s60, 0x1000\nbuffer_load_dwordx4 v[108:111], v87, s[12:15], s101 offen offset:1024 sc1\nv_exp_f32 v12, v12\nv_exp_f32 v13, v13\n"
"v_exp_f32 v14, v14\nv_exp_f32 v15, v15\nv_exp_f32 v16, v16\nv_exp_f32 v17, v17\nv_exp_f32 v18, v18\nv_exp_f32 v19, v19\ns_waitcnt lgkmcnt(0)\n"
"s_barrier\nds_read_b128 v[56:59], v87 offset:32768\nv_smfmac_f32_32x32x32_bf16 v[32:47], v[64:67], v[8:15], v52\ns_setprio 3\n"
"v_add_u32 v72, v72, v73\nv_exp_f32 v20, v20\nv_exp_f32 v21, v21\nv_exp_f32 v22, v22\nv_exp_f32 v23, v23\ns_waitcnt lgkmcnt(0)\n"
"v_mfma_f32_32x32x16_bf16 v[0:15], v[56:59], v[68:71], 0\nds_read_b128 v[56:59], v87 offset:33792\ns_add_u32 s101, s58, 0x5000\n"
"buffer_load_dwordx4 v[216:219], v79, s[12:15], s101 offen offset:2048 sc1\nv_exp_f32 v24, v24\nv_exp_f32 v25, v25\nv_exp_f32 v26, v26\n"
"v_exp_f32 v27, v27\nv_smfmac_f32_32x32x32_bf16 v[32:47], v[60:63], v[16:23], v52\nds_read_b128 v[60:63], v72 offset:0\ns_add_u32 s101, s60, 0x1000\n"
"buffer_load_dwordx4 v[112:115], v87, s[12:15], s101 offen offset:2048 sc1\ns_lshl_b32 s47, s57, 13\ns_add_u32 s47, s47, s61\nv_add_u32 v74, s47, v87\n"
"s_mul_i32 s47, s42, 3072\ns_add_u32 s47, s47, 0x21000\nv_add_u32 v75, s47, v87\nv_exp_f32 v28, v28\nv_exp_f32 v29, v29\nv_exp_f32 v30, v30\n"
"v_exp_f32 v31, v31\nv_exp_f32 v0, v0\nv_exp_f32 v1, v1\nv_exp_f32 v2, v2\nv_exp_f32 v3, v3\ns_waitcnt lgkmcnt(0)\n"
"v_smfmac_f32_32x32x32_bf16 v[32:47], v[80:83], v[24:31], v52\nds_read_b128 v[64:67], v72 offset:512\nv_exp_f32 v4, v4\nv_exp_f32 v5, v5\n"
"v_exp_f32 v6, v6\nv_exp_f32 v7, v7\nv_mfma_f32_32x32x16_bf16 v[16:31], v[56:59], v[68:71], 0\nds_read_b128 v[56:59], v87 offset:34816\n"
"s_add_u32 s101, s58, 0x5000\nbuffer_load_dwordx4 v[220:223], v79, s[12:15], s101 offen offset:3072 sc1\nv_exp_f32 v8, v8\nv_exp_f32 v9, v9\n"
"v_exp_f32 v10, v10\nv_exp_f32 v11, v11\nv_smfmac_f32_32x32x32_bf16 v[32:47], v[60:63], v[0:7], v52\nds_read_b128 v[60:63], v72 offset:1024\n"
"s_add_u32 s101, s60, 0x1000\nbuffer_load_dwordx4 v[116:119], v87, s[12:15], s101 offen offset:3072 sc1\nv_exp_f32 v12, v12\nv_exp_f32 v13, v13\n"
"v_exp_f32 v14, v14\nv_exp_f32 v15, v15\ns_and_b32 s47, s2, 0xf1\ns_xor_b32 s48, s42, 7\ns_or_b32 s47, s47, s48\ns_cmp_lg_u32 s47, 0\n"
"s_cbranch_scc1 .Lpq1\ns_mov_b64 s[30:31], -1\ns_mov_b64 exec, 1\ns_setprio 3\ns_waitcnt vmcnt(0)\ns_memtime s[24:25]\ns_waitcnt lgkmcnt(0)\n"
"buffer_load_dword v77, off, s[12:15], s28 offset:0 sc1\ns_waitcnt vmcnt(0)\ns_memtime s[26:27]\ns_waitcnt lgkmcnt(0)\ns_sub_u32 s47, s26, s24\n"
"s_min_u32 s30, s30, s47\nbuffer_load_dword v77, off, s[12:15], s29 offset:0 sc1\ns_waitcnt vmcnt(0)\ns_memtime s[24:25]\ns_waitcnt lgkmcnt(0)\n"
"s_sub_u32 s47, s24, s26\ns_min_u32 s31, s31, s47\nbuffer_load_dword v77, off, s[12:15], s29 offset:128 sc1\ns_waitcnt vmcnt(0)\ns_memtime s[26:27]\n"
"s_waitcnt lgkmcnt(0)\ns_sub_u32 s47, s26, s24\ns_min_u32 s31, s31, s47\nbuffer_load_dword v77, off, s[12:15], s28 offset:128 sc1\ns_waitcnt vmcnt(0)\n"
"s_memtime s[24:25]\ns_waitcnt lgkmcnt(0)\ns_sub_u32 s47, s24, s26\ns_min_u32 s30, s30, s47\ns_cmp_lt_u32 s31, s30\ns_cselect_b32 s30, 1, 0\n"
"s_bfe_u32 s47, s2, 0x10002\ns_xor_b32 s30, s30, s47\nv_mov_b32 v76, s30\nv_mov_b32 v77, s40\ns_lshl_b32 s47, s2, 6\ns_add_u32 s47, s47, 0xa80000\n"
"buffer_store_dwordx2 v[76:77], off, s[12:15], s47 sc1\ns_mov_b64 exec, -1\ns_setprio 3\ns_nop 0\n.Lpq1:\nv_exp_f32 v16, v16\nv_exp_f32 v17, v17\n"
"v_exp_f32 v18, v18\nv_exp_f32 v19, v19\ns_waitcnt lgkmcnt(0)\nv_smfmac_f32_32x32x32_bf16 v[32:47], v[64:67], v[8:15], v52\n"
"ds_read_b128 v[64:67], v72 offset:1536\ns_waitcnt vmcnt(14)\nds_write_b128 v74, v[88:91] offset:0\nv_exp_f32 v20, v20\nv_exp_f32 v21, v21\n"
"v_exp_f32 v22, v22\nv_exp_f32 v23, v23\nv_mfma_f32_32x32x16_bf16 v[0:15], v[56:59], v[68:71], 0\nds_read_b128 v[56:59], v87 offset:35840\n"
"s_add_u32 s101, s58, 0x6000\nbuffer_load_dwordx4 v[224:227], v79, s[12:15], s101 offen offset:0 sc1\nv_exp_f32 v24, v24\nv_exp_f32 v25, v25\n"
"v_exp_f32 v26, v26\nv_exp_f32 v27, v27\nv_smfmac_f32_32x32x32_bf16 v[32:47], v[60:63], v[16:23], v52\nds_read_b128 v[60:63], v72 offset:2048\n"
"s_add_u32 s101, s59, 0x6000\nbuffer_load_dwordx4 v[180:183], v87, s[12:15], s101 offen offset:1024 sc1\nv_exp_f32 v28, v28\nv_exp_f32 v29, v29\n"
"v_exp_f32 v30, v30\nv_exp_f32 v31, v31\nv_exp_f32 v0, v0\nv_exp_f32 v1, v1\nv_exp_f32 v2, v2\nv_exp_f32 v3, v3\ns_waitcnt lgkmcnt(0)\n"
"v_smfmac_f32_32x32x32_bf16 v[32:47], v[64:67], v[24:31], v52\nds_read_b128 v[64:67], v72 offset:2560\ns_waitcnt vmcnt(14)\n"
"ds_write_b128 v74, v[92:95] offset:1024\nv_exp_f32 v4, v4\nv_exp_f32 v5, v5\nv_exp_f32 v6, v6\nv_exp_f32 v7, v7\n"
"v_mfma_f32_32x32x16_bf16 v[16:31], v[56:59], v[68:71], 0\nds_read_b128 v[56:59], v87 offset:36864\ns_add_u32 s101, s58, 0x6000\n"
"buffer_load_dwordx4 v[228:231], v79, s[12:15], s101 offen offset:1024 sc1\nv_exp_f32 v8, v8\nv_exp_f32 v9, v9\nv_exp_f32 v10, v10\n"
"v_exp_f32 v11, v11\nv_smfmac_f32_32x32x32_bf16 v[32:47], v[60:63], v[0:7], v52\nds_read_b128 v[60:63], v72 offset:3072\ns_add_u32 s101, s59, 0x6000\n"
"buffer_load_dwordx4 v[184:187], v87, s[12:15], s101 offen offset:2048 sc1\nv_exp_f32 v12, v12\nv_exp_f32 v13, v13\nv_exp_f32 v14, v14\n"
"v_exp_f32 v15, v15\nv_exp_f32 v16, v16\nv_exp_f32 v17, v17\nv_exp_f32 v18, v18\nv_exp_f32 v19, v19\ns_waitcnt lgkmcnt(0)\n"
"v_smfmac_f32_32x32x32_bf16 v[32:47], v[64:67], v[8:15], v52\nds_read_b128 v[64:67], v72 offset:3584\ns_waitcnt vmcnt(14)\n"
"ds_write_b128 v74, v[96:99] offset:2048\nv_add_u32 v72, v72, v73\nv_exp_f32 v20, v20\nv_exp_f32 v21, v21\nv_exp_f32 v22, v22\nv_exp_f32 v23, v23\n"
"v_mfma_f32_32x32x16_bf16 v[0:15], v[56:59], v[68:71], 0\nds_read_b128 v[56:59], v87 offset:37888\ns_add_u32 s101, s58, 0x6000\n"
"buffer_load_dwordx4 v[232:235], v79, s[12:15], s101 offen offset:2048 sc1\nv_exp_f32 v24, v24\nv_exp_f32 v25, v25\nv_exp_f32 v26, v26\n"
"v_exp_f32 v27, v27\nv_smfmac_f32_32x32x32_bf16 v[32:47], v[60:63], v[16:23], v52\nds_read_b128 v[60:63], v72 offset:0\ns_add_u32 s101, s59, 0x6000\n"
"buffer_load_dwordx4 v[188:191], v87, s[12:15], s101 offen offset:3072 sc1\nv_exp_f32 v28, v28\nv_exp_f32 v29, v29\nv_exp_f32 v30, v30\n"
"v_exp_f32 v31, v31\nv_exp_f32 v0, v0\nv_exp_f32 v1, v1\nv_exp_f32 v2, v2\nv_exp_f32 v3, v3\ns_waitcnt lgkmcnt(0)\n"
"v_smfmac_f32_32x32x32_bf16 v[32:47], v[64:67], v[24:31], v52\nds_read_b128 v[64:67], v72 offset:512\ns_waitcnt vmcnt(14)\n"
"ds_write_b128 v74, v[100:103] offset:3072\nv_exp_f32 v4, v4\nv_exp_f32 v5, v5\nv_exp_f32 v6, v6\nv_exp_f32 v7, v7\n"
"v_mfma_f32_32x32x16_bf16 v[16:31], v[56:59], v[68:71], 0\nds_read_b128 v[56:59], v87 offset:38912\ns_add_u32 s101, s58, 0x6000\n"
"buffer_load_dwordx4 v[236:239], v79, s[12:15], s101 offen offset:3072 sc1\nv_exp_f32 v8, v8\nv_exp_f32 v9, v9\nv_exp_f32 v10, v10\n"
"v_exp_f32 v11, v11\nv_smfmac_f32_32x32x32_bf16 v[32:47], v[60:63], v[0:7], v52\nds_read_b128 v[60:63], v72 offset:1024\nv_exp_f32 v12, v12\n"
"v_exp_f32 v13, v13\nv_exp_f32 v14, v14\nv_exp_f32 v15, v15\nv_exp_f32 v16, v16\nv_exp_f32 v17, v17\nv_exp_f32 v18, v18\nv_exp_f32 v19, v19\n"
"s_waitcnt lgkmcnt(0)\nv_smfmac_f32_32x32x32_bf16 v[32:47], v[64:67], v[8:15], v52\nds_read_b128 v[64:67], v72 offset:1536\ns_waitcnt vmcnt(13)\n"
"ds_write_b128 v74, v[104:107] offset:4096\nv_exp_f32 v20, v20\nv_exp_f32 v21, v21\nv_exp_f32 v22, v22\nv_exp_f32 v23, v23\n"
"v_mfma_f32_32x32x16_bf16 v[0:15], v[56:59], v[68:71], 0\nds_read_b128 v[56:59], v87 offset:39936\ns_add_u32 s101, s58, 0x7000\n"
"buffer_load_dwordx4 v[240:243], v79, s[12:15], s101 offen offset:0 sc1\nv_exp_f32 v24, v24\nv_exp_f32 v25, v25\nv_exp_f32 v26, v26\n"
"v_exp_f32 v27, v27\nv_smfmac_f32_32x32x32_bf16 v[32:47], v[60:63], v[16:23], v52\nds_read_b128 v[60:63], v72 offset:2048\nv_exp_f32 v28, v28\n"
"v_exp_f32 v29, v29\nv_exp_f32 v30, v30\nv_exp_f32 v31, v31\nv_exp_f32 v0, v0\nv_exp_f32 v1, v1\nv_exp_f32 v2, v2\nv_exp_f32 v3, v3\n"
"s_waitcnt lgkmcnt(0)\nv_smfmac_f32_32x32x32_bf16 v[32:47], v[64:67], v[24:31], v52\nds_read_b128 v[64:67], v72 offset:2560\ns_waitcnt vmcnt(12)\n"
"ds_write_b128 v74, v[108:111] offset:5120\nv_exp_f32 v4, v4\nv_exp_f32 v5, v5\nv_exp_f32 v6, v6\nv_exp_f32 v7, v7\n"
"v_mfma_f32_32x32x16_bf16 v[16:31], v[56:59], v[68:71], 0\nds_read_b128 v[56:59], v87 offset:40960\ns_add_u32 s101, s58, 0x7000\n"
"buffer_load_dwordx4 v[244:247], v79, s[12:15], s101 offen offset:1024 sc1\nv_exp_f32 v8, v8\nv_exp_f32 v9, v9\nv_exp_f32 v10, v10\n"
"v_exp_f32 v11, v11\nv_smfmac_f32_32x32x32_bf16 v[32:47], v[60:63], v[0:7], v52\nds_read_b128 v[60:63], v72 offset:3072\ns_add_u32 s101, s59, 0x0\n"
"buffer_load_dwordx4 v[88:91], v87, s[12:15], s101 offen offset:0 sc1\nv_exp_f32 v12, v12\nv_exp_f32 v13, v13\nv_exp_f32 v14, v14\nv_exp_f32 v15, v15\n"
"v_exp_f32 v16, v16\nv_exp_f32 v17, v17\nv_exp_f32 v18, v18\nv_exp_f32 v19, v19\ns_waitcnt lgkmcnt(0)\n"
"v_smfmac_f32_32x32x32_bf16 v[32:47], v[64:67], v[8:15], v52\nds_read_b128 v[64:67], v72 offset:3584\ns_waitcnt vmcnt(12)\n"
"ds_write_b128 v74, v[112:115] offset:6144\nv_add_u32 v72, v72, v73\nv_exp_f32 v20, v20\nv_exp_f32 v21, v21\nv_exp_f32 v22, v22\nv_exp_f32 v23, v23\n"
"v_mfma_f32_32x32x16_bf16 v[0:15], v[56:59], v[68:71], 0\nds_read_b128 v[56:59], v87 offset:41984\ns_add_u32 s101, s58, 0x7000\n"
"buffer_load_dwordx4 v[248:251], v79, s[12:15], s101 offen offset:2048 sc1\nv_exp_f32 v24, v24\nv_exp_f32 v25, v25\nv_exp_f32 v26, v26\n"
"v_exp_f32 v27, v27\nv_smfmac_f32_32x32x32_bf16 v[32:47], v[60:63], v[16:23], v52\nds_read_b128 v[60:63], v72 offset:0\ns_add_u32 s101, s59, 0x0\n"
"buffer_load_dwordx4 v[92:95], v87, s[12:15], s101 offen offset:1024 sc1\nv_exp_f32 v28, v28\nv_exp_f32 v29, v29\nv_exp_f32 v30, v30\n"
"v_exp_f32 v31, v31\nv_exp_f32 v0, v0\nv_exp_f32 v1, v1\nv_exp_f32 v2, v2\nv_exp_f32 v3, v3\ns_waitcnt lgkmcnt(0)\n"
"v_smfmac_f32_32x32x32_bf16 v[32:47], v[64:67], v[24:31], v52\nds_read_b128 v[64:67], v72 offset:512\ns_waitcnt vmcnt(12)\n"
"ds_write_b128 v74, v[116:119] offset:7168\nv_exp_f32 v4, v4\nv_exp_f32 v5, v5\nv_exp_f32 v6, v6\nv_exp_f32 v7, v7\n"
"v_mfma_f32_32x32x16_bf16 v[16:31], v[56:59], v[68:71], 0\nds_read_b128 v[56:59], v87 offset:43008\ns_add_u32 s101, s58, 0x7000\n"
"buffer_load_dwordx4 v[252:255], v79, s[12:15], s101 offen offset:3072 sc1\nv_exp_f32 v8, v8\nv_exp_f32 v9, v9\nv_exp_f32 v10, v10\n"
"v_exp_f32 v11, v11\nv_smfmac_f32_32x32x32_bf16 v[32:47], v[60:63], v[0:7], v52\nds_read_b128 v[60:63], v72 offset:1024\ns_add_u32 s101, s59, 0x0\n"
"buffer_load_dwordx4 v[96:99], v87, s[12:15], s101 offen offset:2048 sc1\nv_exp_f32 v12, v12\nv_exp_f32 v13, v13\nv_exp_f32 v14, v14\n"
"v_exp_f32 v15, v15\nv_exp_f32 v16, v16\nv_exp_f32 v17, v17\nv_exp_f32 v18, v18\nv_exp_f32 v19, v19\ns_waitcnt lgkmcnt(0)\n"
"v_smfmac_f32_32x32x32_bf16 v[32:47], v[64:67], v[8:15], v52\nds_read_b128 v[64:67], v72 offset:1536\ns_waitcnt vmcnt(12)\n"
"ds_write_b128 v75, v[180:183] offset:0\nv_exp_f32 v20, v20\nv_exp_f32 v21, v21\nv_exp_f32 v22, v22\nv_exp_f32 v23, v23\n"
"v_mfma_f32_32x32x16_bf16 v[0:15], v[56:59], v[68:71], 0\nds_read_b128 v[56:59], v87 offset:44032\ns_add_u32 s101, s58, 0x0\n"
"buffer_load_dwordx4 v[128:131], v79, s[12:15], s101 offen offset:0 sc1\nv_exp_f32 v24, v24\nv_exp_f32 v25, v25\nv_exp_f32 v26, v26\n"
"v_exp_f32 v27, v27\nv_smfmac_f32_32x32x32_bf16 v[32:47], v[60:63], v[16:23], v52\nds_read_b128 v[60:63], v72 offset:2048\ns_add_u32 s101, s59, 0x0\n"
"buffer_load_dwordx4 v[100:103], v87, s[12:15], s101 offen offset:3072 sc1\nv_exp_f32 v28, v28\nv_exp_f32 v29, v29\nv_exp_f32 v30, v30\n"
"v_exp_f32 v31, v31\nv_exp_f32 v0, v0\nv_exp_f32 v1, v1\nv_exp_f32 v2, v2\nv_exp_f32 v3, v3\ns_waitcnt lgkmcnt(0)\n"
"v_smfmac_f32_32x32x32_bf16 v[32:47], v[64:67], v[24:31], v52\nds_read_b128 v[64:67], v72 offset:2560\ns_waitcnt vmcnt(12)\n"
"ds_write_b128 v75, v[184:187] offset:1024\nv_exp_f32 v4, v4\nv_exp_f32 v5, v5\nv_exp_f32 v6, v6\nv_exp_f32 v7, v7\n"
"v_mfma_f32_32x32x16_bf16 v[16:31], v[56:59], v[68:71], 0\nds_read_b128 v[56:59], v87 offset:45056\ns_add_u32 s101, s58, 0x0\n"
"buffer_load_dwordx4 v[132:135], v79, s[12:15], s101 offen offset:1024 sc1\nv_exp_f32 v8, v8\nv_exp_f32 v9, v9\nv_exp_f32 v10, v10\n"
"v_exp_f32 v11, v11\nv_smfmac_f32_32x32x32_bf16 v[32:47], v[60:63], v[0:7], v52\nds_read_b128 v[60:63], v72 offset:3072\ns_add_u32 s101, s59, 0x1000\n"
"buffer_load_dwordx4 v[104:107], v87, s[12:15], s101 offen offset:0 sc1\nv_exp_f32 v12, v12\nv_exp_f32 v13, v13\nv_exp_f32 v14, v14\n"
"v_exp_f32 v15, v15\nv_exp_f32 v16, v16\nv_exp_f32 v17, v17\nv_exp_f32 v18, v18\nv_exp_f32 v19, v19\ns_waitcnt lgkmcnt(0)\n"
"v_smfmac_f32_32x32x32_bf16 v[32:47], v[64:67], v[8:15], v52\nds_read_b128 v[64:67], v72 offset:3584\ns_waitcnt vmcnt(12)\n"
"ds_write_b128 v75, v[188:191] offset:2048\nv_add_u32 v72, v72, v73\nv_exp_f32 v20, v20\nv_exp_f32 v21, v21\nv_exp_f32 v22, v22\nv_exp_f32 v23, v23\n"
"v_mfma_f32_32x32x16_bf16 v[0:15], v[56:59], v[68:71], 0\nds_read_b128 v[56:59], v87 offset:46080\ns_add_u32 s101, s58, 0x0\n"
"buffer_load_dwordx4 v[136:139], v79, s[12:15], s101 offen offset:2048 sc1\nv_exp_f32 v24, v24\nv_exp_f32 v25, v25\nv_exp_f32 v26, v26\n"
"v_exp_f32 v27, v27\nv_smfmac_f32_32x32x32_bf16 v[32:47], v[60:63], v[16:23], v52\nds_read_b128 v[60:63], v72 offset:0\ns_add_u32 s101, s59, 0x1000\n"
"buffer_load_dwordx4 v[108:111], v87, s[12:15], s101 offen offset:1024 sc1\nv_exp_f32 v28, v28\nv_exp_f32 v29, v29\nv_exp_f32 v30, v30\n"
"v_exp_f32 v31, v31\nv_exp_f32 v0, v0\nv_exp_f32 v1, v1\nv_exp_f32 v2, v2\nv_exp_f32 v3, v3\ns_waitcnt lgkmcnt(0)\n"
"v_smfmac_f32_32x32x32_bf16 v[32:47], v[64:67], v[24:31], v52\nds_read_b128 v[64:67], v72 offset:512\nv_exp_f32 v4, v4\nv_exp_f32 v5, v5\n"
"v_exp_f32 v6, v6\nv_exp_f32 v7, v7\nv_mfma_f32_32x32x16_bf16 v[16:31], v[56:59], v[68:71], 0\nds_read_b128 v[56:59], v87 offset:47104\n"
"s_add_u32 s101, s58, 0x0\nbuffer_load_dwordx4 v[140:143], v79, s[12:15], s101 offen offset:3072 sc1\nv_exp_f32 v8, v8\nv_exp_f32 v9, v9\n"
"v_exp_f32 v10, v10\nv_exp_f32 v11, v11\nv_smfmac_f32_32x32x32_bf16 v[32:47], v[60:63], v[0:7], v52\nds_read_b128 v[60:63], v72 offset:1024\n"
"v_exp_f32 v12, v12\nv_exp_f32 v13, v13\nv_exp_f32 v14, v14\nv_exp_f32 v15, v15\nv_exp_f32 v16, v16\nv_exp_f32 v17, v17\nv_exp_f32 v18, v18\n"
"v_exp_f32 v19, v19\ns_waitcnt lgkmcnt(0)\nv_smfmac_f32_32x32x32_bf16 v[32:47], v[64:67], v[8:15], v52\nds_read_b128 v[64:67], v72 offset:1536\n"
"v_exp_f32 v20, v20\nv_exp_f32 v21, v21\nv_exp_f32 v22, v22\nv_exp_f32 v23, v23\nv_mfma_f32_32x32x16_bf16 v[0:15], v[56:59], v[68:71], 0\n"
"ds_read_b128 v[56:59], v87 offset:48128\ns_add_u32 s101, s58, 0x1000\nbuffer_load_dwordx4 v[144:147], v79, s[12:15], s101 offen offset:0 sc1\n"
"v_exp_f32 v24, v24\nv_exp_f32 v25, v25\nv_exp_f32 v26, v26\nv_exp_f32 v27, v27\nv_smfmac_f32_32x32x32_bf16 v[32:47], v[60:63], v[16:23], v52\n"
"ds_read_b128 v[60:63], v72 offset:2048\nv_exp_f32 v28, v28\nv_exp_f32 v29, v29\nv_exp_f32 v30, v30\nv_exp_f32 v31, v31\nv_exp_f32 v0, v0\n"
"v_exp_f32 v1, v1\nv_exp_f32 v2, v2\nv_exp_f32 v3, v3\ns_waitcnt lgkmcnt(0)\nv_smfmac_f32_32x32x32_bf16 v[32:47], v[64:67], v[24:31], v52\n"
"ds_read_b128 v[64:67], v72 offset:2560\nv_exp_f32 v4, v4\nv_exp_f32 v5, v5\nv_exp_f32 v6, v6\nv_exp_f32 v7, v7\n"
"v_mfma_f32_32x32x16_bf16 v[16:31], v[56:59], v[68:71], 0\nds_read_b128 v[56:59], v87 offset:49152\ns_add_u32 s101, s58, 0x1000\n"
"buffer_load_dwordx4 v[148:151], v79, s[12:15], s101 offen offset:1024 sc1\nv_exp_f32 v8, v8\nv_exp_f32 v9, v9\nv_exp_f32 v10, v10\n"
"v_exp_f32 v11, v11\nv_smfmac_f32_32x32x32_bf16 v[32:47], v[60:63], v[0:7], v52\nds_read_b128 v[60:63], v72 offset:3072\nv_exp_f32 v12, v12\n"
"v_exp_f32 v13, v13\nv_exp_f32 v14, v14\nv_exp_f32 v15, v15\nv_exp_f32 v16, v16\nv_exp_f32 v17, v17\nv_exp_f32 v18, v18\nv_exp_f32 v19, v19\n"
"s_waitcnt lgkmcnt(0)\nv_smfmac_f32_32x32x32_bf16 v[32:47], v[64:67], v[8:15], v52\nds_read_b128 v[64:67], v72 offset:3584\nv_add_u32 v72, v72, v73\n"
"v_exp_f32 v20, v20\nv_exp_f32 v21, v21\nv_exp_f32 v22, v22\nv_exp_f32 v23, v23\nv_mfma_f32_32x32x16_bf16 v[0:15], v[56:59], v[68:71], 0\n"
"ds_read_b128 v[56:59], v87 offset:50176\ns_add_u32 s101, s58, 0x1000\nbuffer_load_dwordx4 v[152:155], v79, s[12:15], s101 offen offset:2048 sc1\n"
"v_exp_f32 v24, v24\nv_exp_f32 v25, v25\nv_exp_f32 v26, v26\nv_exp_f32 v27, v27\nv_smfmac_f32_32x32x32_bf16 v[32:47], v[60:63], v[16:23], v52\n"
"ds_read_b128 v[60:63], v72 offset:0\nv_bfe_u32 v76, v87, 4, 5\nv_lshrrev_b32 v77, 9, v87\nv_lshlrev_b32 v78, 10, v76\n"
"v_lshl_add_u32 v78, v77, 3, v78\ns_lshl1_add_u32 s62, s43, s57\ns_lshl_b32 s62, s62, 15\ns_lshl2_add_u32 s63, s44, s56\ns_lshl_b32 s47, s63, 5\n"
"s_add_u32 s47, s47, s62\nbuffer_load_dwordx2 v[80:81], v78, s[4:7], s47 offen\nbuffer_load_dwordx2 v[82:83], v78, s[4:7], s47 offen offset:16\n"
"v_exp_f32 v28, v28\nv_exp_f32 v29, v29\nv_exp_f32 v30, v30\nv_exp_f32 v31, v31\nv_exp_f32 v0, v0\nv_exp_f32 v1, v1\nv_exp_f32 v2, v2\n"
"v_exp_f32 v3, v3\ns_waitcnt lgkmcnt(0)\nv_smfmac_f32_32x32x32_bf16 v[32:47], v[64:67], v[24:31], v52\nds_read_b128 v[64:67], v72 offset:512\n"
"v_exp_f32 v4, v4\nv_exp_f32 v5, v5\nv_exp_f32 v6, v6\nv_exp_f32 v7, v7\nv_mfma_f32_32x32x16_bf16 v[16:31], v[56:59], v[68:71], 0\n"
"ds_read_b128 v[56:59], v87 offset:51200\ns_add_u32 s101, s58, 0x1000\nbuffer_load_dwordx4 v[156:159], v79, s[12:15], s101 offen offset:3072 sc1\n"
"v_exp_f32 v8, v8\nv_exp_f32 v9, v9\nv_exp_f32 v10, v10\nv_exp_f32 v11, v11\nv_smfmac_f32_32x32x32_bf16 v[32:47], v[60:63], v[0:7], v52\n"
"ds_read_b128 v[60:63], v72 offset:1024\nv_lshlrev_b32 v76, 1, v76\nv_lshl_add_u32 v76, v77, 16, v76\ns_mul_i32 s47, s44, 7\ns_add_u32 s47, s47, s42\n"
"s_lshl_b32 s47, s47, 6\nbuffer_load_ushort v86, v76, s[32:35], s47 offen\nbuffer_load_dword v84, v87, s[36:39], 0 offen\nv_exp_f32 v12, v12\n"
"v_exp_f32 v13, v13\nv_exp_f32 v14, v14\nv_exp_f32 v15, v15\nv_exp_f32 v16, v16\nv_exp_f32 v17, v17\nv_exp_f32 v18, v18\nv_exp_f32 v19, v19\n"
"s_waitcnt lgkmcnt(0)\nv_smfmac_f32_32x32x32_bf16 v[32:47], v[64:67], v[8:15], v52\nds_read_b128 v[64:67], v72 offset:1536\nv_exp_f32 v20, v20\n"
"v_exp_f32 v21, v21\nv_exp_f32 v22, v22\nv_exp_f32 v23, v23\nv_mfma_f32_32x32x16_bf16 v[0:15], v[56:59], v[68:71], 0\n"
"ds_read_b128 v[56:59], v87 offset:52224\ns_add_u32 s101, s58, 0x2000\nbuffer_load_dwordx4 v[160:163], v79, s[12:15], s101 offen offset:0 sc1\n"
"v_exp_f32 v24, v24\nv_exp_f32 v25, v25\nv_exp_f32 v26, v26\nv_exp_f32 v27, v27\nv_smfmac_f32_32x32x32_bf16 v[32:47], v[60:63], v[16:23], v52\n"
"ds_read_b128 v[60:63], v72 offset:2048\nv_exp_f32 v28, v28\nv_exp_f32 v29, v29\nv_exp_f32 v30, v30\nv_exp_f32 v31, v31\nv_exp_f32 v0, v0\n"
"v_exp_f32 v1, v1\nv_exp_f32 v2, v2\nv_exp_f32 v3, v3\ns_waitcnt lgkmcnt(0)\nv_smfmac_f32_32x32x32_bf16 v[32:47], v[64:67], v[24:31], v52\n"
"ds_read_b128 v[64:67], v72 offset:2560\nv_exp_f32 v4, v4\nv_exp_f32 v5, v5\nv_exp_f32 v6, v6\nv_exp_f32 v7, v7\n"
"v_mfma_f32_32x32x16_bf16 v[16:31], v[56:59], v[68:71], 0\nds_read_b128 v[56:59], v87 offset:53248\ns_add_u32 s101, s58, 0x2000\n"
"buffer_load_dwordx4 v[164:167], v79, s[12:15], s101 offen offset:1024 sc1\nv_exp_f32 v8, v8\nv_exp_f32 v9, v9\nv_exp_f32 v10, v10\n"
"v_exp_f32 v11, v11\nv_smfmac_f32_32x32x32_bf16 v[32:47], v[60:63], v[0:7], v52\nds_read_b128 v[60:63], v72 offset:3072\nv_exp_f32 v12, v12\n"
"v_exp_f32 v13, v13\nv_exp_f32 v14, v14\nv_exp_f32 v15, v15\nv_exp_f32 v16, v16\nv_exp_f32 v17, v17\nv_exp_f32 v18, v18\nv_exp_f32 v19, v19\n"
"s_waitcnt lgkmcnt(0)\nv_smfmac_f32_32x32x32_bf16 v[32:47], v[64:67], v[8:15], v52\nds_read_b128 v[64:67], v72 offset:3584\nv_add_u32 v72, v72, v73\n"
"v_exp_f32 v20, v20\nv_exp_f32 v21, v21\nv_exp_f32 v22, v22\nv_exp_f32 v23, v23\nv_mfma_f32_32x32x16_bf16 v[0:15], v[56:59], v[68:71], 0\n"
"ds_read_b128 v[56:59], v87 offset:54272\ns_add_u32 s101, s58, 0x2000\nbuffer_load_dwordx4 v[168:171], v79, s[12:15], s101 offen offset:2048 sc1\n"
"v_exp_f32 v24, v24\nv_exp_f32 v25, v25\nv_exp_f32 v26, v26\nv_exp_f32 v27, v27\nv_smfmac_f32_32x32x32_bf16 v[32:47], v[60:63], v[16:23], v52\n"
"ds_read_b128 v[60:63], v72 offset:0\nv_exp_f32 v28, v28\nv_exp_f32 v29, v29\nv_exp_f32 v30, v30\nv_exp_f32 v31, v31\nv_exp_f32 v0, v0\n"
"v_exp_f32 v1, v1\nv_exp_f32 v2, v2\nv_exp_f32 v3, v3\ns_waitcnt lgkmcnt(0)\nv_smfmac_f32_32x32x32_bf16 v[32:47], v[64:67], v[24:31], v52\n"
"ds_read_b128 v[64:67], v72 offset:512\nv_exp_f32 v4, v4\nv_exp_f32 v5, v5\nv_exp_f32 v6, v6\nv_exp_f32 v7, v7\n"
"v_mfma_f32_32x32x16_bf16 v[16:31], v[56:59], v[68:71], 0\nds_read_b128 v[56:59], v87 offset:55296\ns_add_u32 s101, s58, 0x2000\n"
"buffer_load_dwordx4 v[172:175], v79, s[12:15], s101 offen offset:3072 sc1\nv_exp_f32 v8, v8\nv_exp_f32 v9, v9\nv_exp_f32 v10, v10\n"
"v_exp_f32 v11, v11\nv_smfmac_f32_32x32x32_bf16 v[32:47], v[60:63], v[0:7], v52\nds_read_b128 v[60:63], v72 offset:1024\nv_exp_f32 v12, v12\n"
"v_exp_f32 v13, v13\nv_exp_f32 v14, v14\nv_exp_f32 v15, v15\nv_exp_f32 v16, v16\nv_exp_f32 v17, v17\nv_exp_f32 v18, v18\nv_exp_f32 v19, v19\n"
"s_waitcnt lgkmcnt(0)\nv_smfmac_f32_32x32x32_bf16 v[32:47], v[64:67], v[8:15], v52\nds_read_b128 v[64:67], v72 offset:1536\nv_exp_f32 v20, v20\n"
"v_exp_f32 v21, v21\nv_exp_f32 v22, v22\nv_exp_f32 v23, v23\nv_mfma_f32_32x32x16_bf16 v[0:15], v[56:59], v[68:71], 0\n"
"ds_read_b128 v[56:59], v87 offset:56320\ns_add_u32 s101, s58, 0x3000\nbuffer_load_dwordx4 v[176:179], v79, s[12:15], s101 offen offset:0 sc1\n"
"v_exp_f32 v24, v24\nv_exp_f32 v25, v25\nv_exp_f32 v26, v26\nv_exp_f32 v27, v27\nv_smfmac_f32_32x32x32_bf16 v[32:47], v[60:63], v[16:23], v52\n"
"ds_read_b128 v[60:63], v72 offset:2048\nv_exp_f32 v28, v28\nv_exp_f32 v29, v29\nv_exp_f32 v30, v30\nv_exp_f32 v31, v31\nv_exp_f32 v0, v0\n"
"v_exp_f32 v1, v1\nv_exp_f32 v2, v2\nv_exp_f32 v3, v3\ns_waitcnt lgkmcnt(0)\nv_smfmac_f32_32x32x32_bf16 v[32:47], v[64:67], v[24:31], v52\n"
"ds_read_b128 v[64:67], v72 offset:2560\nv_exp_f32 v4, v4\nv_exp_f32 v5, v5\nv_exp_f32 v6, v6\nv_exp_f32 v7, v7\n"
"v_mfma_f32_32x32x16_bf16 v[16:31], v[56:59], v[68:71], 0\nds_read_b128 v[56:59], v87 offset:57344\ns_add_u32 s101, s58, 0x3000\n"
"buffer_load_dwordx4 v[180:183], v79, s[12:15], s101 offen offset:1024 sc1\nv_exp_f32 v8, v8\nv_exp_f32 v9, v9\nv_exp_f32 v10, v10\n"
"v_exp_f32 v11, v11\nv_smfmac_f32_32x32x32_bf16 v[32:47], v[60:63], v[0:7], v52\nds_read_b128 v[60:63], v72 offset:3072\nv_exp_f32 v12, v12\n"
"v_exp_f32 v13, v13\nv_exp_f32 v14, v14\nv_exp_f32 v15, v15\nv_exp_f32 v16, v16\nv_exp_f32 v17, v17\nv_exp_f32 v18, v18\nv_exp_f32 v19, v19\n"
"s_waitcnt lgkmcnt(0)\nv_smfmac_f32_32x32x32_bf16 v[32:47], v[64:67], v[8:15], v52\nds_read_b128 v[64:67], v72 offset:3584\nv_add_u32 v72, v72, v73\n"
"v_exp_f32 v20, v20\nv_exp_f32 v21, v21\nv_exp_f32 v22, v22\nv_exp_f32 v23, v23\nv_mfma_f32_32x32x16_bf16 v[0:15], v[56:59], v[68:71], 0\n"
"ds_read_b128 v[56:59], v87 offset:58368\ns_add_u32 s101, s58, 0x3000\nbuffer_load_dwordx4 v[184:187], v79, s[12:15], s101 offen offset:2048 sc1\n"
"v_exp_f32 v24, v24\nv_exp_f32 v25, v25\nv_exp_f32 v26, v26\nv_exp_f32 v27, v27\nv_smfmac_f32_32x32x32_bf16 v[32:47], v[60:63], v[16:23], v52\n"
"ds_read_b128 v[60:63], v72 offset:0\nv_exp_f32 v28, v28\nv_exp_f32 v29, v29\nv_exp_f32 v30, v30\nv_exp_f32 v31, v31\nv_exp_f32 v0, v0\n"
"v_exp_f32 v1, v1\nv_exp_f32 v2, v2\nv_exp_f32 v3, v3\ns_waitcnt lgkmcnt(0)\nv_smfmac_f32_32x32x32_bf16 v[32:47], v[64:67], v[24:31], v52\n"
"ds_read_b128 v[64:67], v72 offset:512\ns_setprio 2\nv_exp_f32 v4, v4\nv_exp_f32 v5, v5\nv_exp_f32 v6, v6\nv_exp_f32 v7, v7\n"
"v_mfma_f32_32x32x16_bf16 v[16:31], v[56:59], v[68:71], 0\nds_read_b128 v[56:59], v87 offset:59392\ns_add_u32 s101, s58, 0x3000\n"
"buffer_load_dwordx4 v[188:191], v79, s[12:15], s101 offen offset:3072 sc1\nv_exp_f32 v8, v8\nv_exp_f32 v9, v9\nv_exp_f32 v10, v10\n"
"v_exp_f32 v11, v11\nv_smfmac_f32_32x32x32_bf16 v[32:47], v[60:63], v[0:7], v52\nds_read_b128 v[60:63], v72 offset:1024\nv_exp_f32 v12, v12\n"
"v_exp_f32 v13, v13\nv_exp_f32 v14, v14\nv_exp_f32 v15, v15\nv_exp_f32 v16, v16\nv_exp_f32 v17, v17\nv_exp_f32 v18, v18\nv_exp_f32 v19, v19\n"
"s_waitcnt lgkmcnt(0)\nv_smfmac_f32_32x32x32_bf16 v[32:47], v[64:67], v[8:15], v52\nds_read_b128 v[64:67], v72 offset:1536\nv_exp_f32 v20, v20\n"
"v_exp_f32 v21, v21\nv_exp_f32 v22, v22\nv_exp_f32 v23, v23\nv_mfma_f32_32x32x16_bf16 v[0:15], v[56:59], v[68:71], 0\n"
"ds_read_b128 v[56:59], v87 offset:60416\nv_exp_f32 v24, v24\nv_exp_f32 v25, v25\nv_exp_f32 v26, v26\nv_exp_f32 v27, v27\n"
"v_smfmac_f32_32x32x32_bf16 v[32:47], v[60:63], v[16:23], v52\nds_read_b128 v[60:63], v72 offset:2048\nv_lshlrev_b32 v78, 3, v87\n"
"v_and_b32 v78, 0x3ff, v78\nv_add_u32 v78, 0xa80000, v78\nbuffer_load_dwordx2 v[76:77], v78, s[12:15], 0 offen sc1\nv_exp_f32 v28, v28\n"
"v_exp_f32 v29, v29\nv_exp_f32 v30, v30\nv_exp_f32 v31, v31\nv_exp_f32 v0, v0\nv_exp_f32 v1, v1\nv_exp_f32 v2, v2\nv_exp_f32 v3, v3\n"
"s_waitcnt lgkmcnt(0)\nv_smfmac_f32_32x32x32_bf16 v[32:47], v[64:67], v[24:31], v52\nds_read_b128 v[64:67], v72 offset:2560\nv_exp_f32 v4, v4\n"
"v_exp_f32 v5, v5\nv_exp_f32 v6, v6\nv_exp_f32 v7, v7\nv_mfma_f32_32x32x16_bf16 v[16:31], v[56:59], v[68:71], 0\n"
"ds_read_b128 v[56:59], v87 offset:61440\nv_exp_f32 v8, v8\nv_exp_f32 v9, v9\nv_exp_f32 v10, v10\nv_exp_f32 v11, v11\n"
"v_smfmac_f32_32x32x32_bf16 v[32:47], v[60:63], v[0:7], v52\nds_read_b128 v[60:63], v72 offset:3072\nv_exp_f32 v12, v12\nv_exp_f32 v13, v13\n"
"v_exp_f32 v14, v14\nv_exp_f32 v15, v15\ns_nop 0\nv_exp_f32 v16, v16\nv_exp_f32 v17, v17\nv_exp_f32 v18, v18\nv_exp_f32 v19, v19\n"
"s_waitcnt lgkmcnt(0)\nv_smfmac_f32_32x32x32_bf16 v[32:47], v[64:67], v[8:15], v52\nds_read_b128 v[64:67], v72 offset:3584\nv_add_u32 v72, v72, v73\n"
"v_exp_f32 v20, v20\nv_exp_f32 v21, v21\nv_exp_f32 v22, v22\nv_exp_f32 v23, v23\nv_mfma_f32_32x32x16_bf16 v[0:15], v[56:59], v[68:71], 0\n"
"ds_read_b128 v[56:59], v87 offset:62464\nv_exp_f32 v24, v24\nv_exp_f32 v25, v25\nv_exp_f32 v26, v26\nv_exp_f32 v27, v27\n"
"v_smfmac_f32_32x32x32_bf16 v[32:47], v[60:63], v[16:23], v52\nds_read_b128 v[60:63], v72 offset:0\nv_exp_f32 v28, v28\nv_exp_f32 v29, v29\n"
"v_exp_f32 v30, v30\nv_exp_f32 v31, v31\ns_nop 0\nv_exp_f32 v0, v0\nv_exp_f32 v1, v1\nv_exp_f32 v2, v2\nv_exp_f32 v3, v3\ns_waitcnt lgkmcnt(0)\n"
"v_smfmac_f32_32x32x32_bf16 v[32:47], v[64:67], v[24:31], v52\nds_read_b128 v[64:67], v72 offset:512\nv_exp_f32 v4, v4\nv_exp_f32 v5, v5\n"
"v_exp_f32 v6, v6\nv_exp_f32 v7, v7\nv_mfma_f32_32x32x16_bf16 v[16:31], v[56:59], v[68:71], 0\nds_read_b128 v[56:59], v87 offset:63488\n"
"v_exp_f32 v8, v8\nv_exp_f32 v9, v9\nv_exp_f32 v10, v10\nv_exp_f32 v11, v11\nv_smfmac_f32_32x32x32_bf16 v[32:47], v[60:63], v[0:7], v52\n"
"ds_read_b128 v[60:63], v72 offset:1024\nv_exp_f32 v12, v12\nv_exp_f32 v13, v13\nv_exp_f32 v14, v14\nv_exp_f32 v15, v15\ns_nop 0\nv_exp_f32 v16, v16\n"
"v_exp_f32 v17, v17\nv_exp_f32 v18, v18\nv_exp_f32 v19, v19\ns_waitcnt lgkmcnt(0)\nv_smfmac_f32_32x32x32_bf16 v[32:47], v[64:67], v[8:15], v52\n"
"ds_read_b128 v[64:67], v72 offset:1536\nv_exp_f32 v20, v20\nv_exp_f32 v21, v21\nv_exp_f32 v22, v22\nv_exp_f32 v23, v23\n"
"v_mfma_f32_32x32x16_bf16 v[0:15], v[56:59], v[68:71], 0\nds_read_b128 v[56:59], v87 offset:64512\nv_exp_f32 v24, v24\nv_exp_f32 v25, v25\n"
"v_exp_f32 v26, v26\nv_exp_f32 v27, v27\nv_smfmac_f32_32x32x32_bf16 v[32:47], v[60:63], v[16:23], v52\nds_read_b128 v[60:63], v72 offset:2048\n"
"s_waitcnt vmcnt(0)\nv_cmp_ne_u32 vcc, s40, v77\ns_mov_b32 s47, 0\ns_cbranch_vccz .Lpq3\ns_nop 6\n.Lpq2:\n"
"buffer_load_dwordx2 v[76:77], v78, s[12:15], 0 offen sc1\ns_waitcnt vmcnt(0)\nv_cmp_ne_u32 vcc, s40, v77\ns_add_u32 s47, s47, 1\n"
"s_cmp_lt_u32 s47, 0x40000\ns_cselect_b64 vcc, vcc, 0\ns_cbranch_vccnz .Lpq2\n.Lpq3:\nv_cmp_ne_u32 vcc, 0, v76\ns_nop 0\ns_xor_b32 s100, vcc_lo, 0x0\n"
"v_exp_f32 v28, v28\nv_exp_f32 v29, v29\nv_exp_f32 v30, v30\nv_exp_f32 v31, v31\ns_and_b32 s52, s44, 3\ns_lshl3_add_u32 s52, s52, s42\n"
"s_lshl_b32 s52, s52, 16\ns_and_b32 s47, s43, 0x1b\ns_lshl_b32 s47, s47, 11\ns_add_u32 s52, s52, s47\ns_bfe_u32 s48, s43, 0x10002\n"
"s_lshl_b32 s47, s48, 21\ns_add_u32 s52, s52, s47\ns_add_u32 s52, s52, 0x1c00000\ns_lshr_b32 s49, s44, 2\ns_lshr_b32 s47, s100, s48\n"
"s_xor_b32 s47, s47, s49\ns_and_b32 s47, s47, 1\ns_lshl_b32 s47, s47, 13\ns_or_b32 s52, s52, s47\ns_bfe_i32 s47, s43, 0x10004\n"
"s_xor_b32 s100, s100, s47\nv_exp_f32 v0, v0\nv_exp_f32 v1, v1\nv_exp_f32 v2, v2\nv_exp_f32 v3, v3\ns_waitcnt lgkmcnt(0)\n"
"v_smfmac_f32_32x32x32_bf16 v[32:47], v[64:67], v[24:31], v52\nds_read_b128 v[64:67], v72 offset:2560\ns_lshl_b32 s47, s56, 14\n"
"s_lshl1_add_u32 s20, s62, s47\ns_bitset0_b32 s20, 21\ns_add_u32 s20, s20, 0x1c00000\ns_add_u32 s22, s20, 0x200000\ns_lshr_b32 s47, s100, 0\n"
"s_and_b32 s47, s47, 1\ns_lshl_b32 s47, s47, 13\ns_or_b32 s20, s20, s47\ns_lshr_b32 s47, s100, 1\ns_and_b32 s47, s47, 1\ns_lshl_b32 s47, s47, 13\n"
"s_or_b32 s22, s22, s47\ns_add_u32 s21, s20, 4096\ns_add_u32 s23, s22, 4096\nv_exp_f32 v4, v4\nv_exp_f32 v5, v5\nv_exp_f32 v6, v6\nv_exp_f32 v7, v7\n"
"v_mfma_f32_32x32x16_bf16 v[16:31], v[56:59], v[68:71], 0\ns_lshl_b32 s47, s63, 10\ns_add_u32 s28, s62, s47\ns_bitset0_b32 s28, 20\n"
"s_and_b32 s47, s28, 0x2000\ns_mul_i32 s47, s47, 127\ns_add_u32 s28, s28, s47\ns_lshr_b32 s47, s100, 6\ns_and_b32 s47, s47, 1\n"
"s_lshl_b32 s47, s47, 13\ns_or_b32 s28, s28, s47\ns_add_u32 s28, s28, 0x2800000\ns_lshl_b32 s29, s43, 10\ns_bitset0_b32 s29, 14\n"
"s_and_b32 s47, s29, 0x2000\ns_mul_i32 s47, s47, 1\ns_add_u32 s29, s29, s47\ns_lshr_b32 s47, s100, 7\ns_and_b32 s47, s47, 1\ns_lshl_b32 s47, s47, 13\n"
"s_or_b32 s29, s29, s47\ns_add_u32 s29, s29, 0x2a00000\nv_exp_f32 v8, v8\nv_exp_f32 v9, v9\nv_exp_f32 v10, v10\nv_exp_f32 v11, v11\n"
"v_smfmac_f32_32x32x32_bf16 v[32:47], v[60:63], v[0:7], v52\nds_read_b128 v[60:63], v72 offset:3072\ns_lshl_b32 s47, s42, 12\ns_lshl_b32 s30, s43, 16\n"
"s_add_u32 s30, s30, s47\ns_bitset0_b32 s30, 20\ns_and_b32 s47, s30, 0x2000\ns_mul_i32 s47, s47, 127\ns_add_u32 s30, s30, s47\n"
"s_lshr_b32 s47, s100, 6\ns_and_b32 s47, s47, 1\ns_lshl_b32 s47, s47, 13\ns_or_b32 s30, s30, s47\ns_add_u32 s30, s30, 0x2800000\n"
"s_and_b32 s31, s43, 15\ns_lshr_b32 s48, s31, 2\ns_add_u32 s48, s48, 2\ns_lshl_b32 s31, s31, 19\ns_lshr_b32 s47, s100, s48\ns_and_b32 s47, s47, 1\n"
"s_lshl_b32 s47, s47, 13\ns_or_b32 s31, s31, s47\ns_add_u32 s31, s31, 0x2000000\nv_exp_f32 v12, v12\nv_exp_f32 v13, v13\nv_exp_f32 v14, v14\n"
"v_exp_f32 v15, v15\nv_exp_f32 v16, v16\nv_exp_f32 v17, v17\nv_exp_f32 v18, v18\nv_exp_f32 v19, v19\ns_waitcnt lgkmcnt(0)\n"
"v_smfmac_f32_32x32x32_bf16 v[32:47], v[64:67], v[8:15], v52\nds_read_b128 v[64:67], v72 offset:3584\ns_setprio 1\nv_add_u32 v72, v72, v73\n"
"v_exp_f32 v20, v20\nv_exp_f32 v21, v21\nv_exp_f32 v22, v22\nv_exp_f32 v23, v23\nv_exp_f32 v24, v24\nv_exp_f32 v25, v25\nv_exp_f32 v26, v26\n"
"v_exp_f32 v27, v27\nv_smfmac_f32_32x32x32_bf16 v[32:47], v[60:63], v[16:23], v52\ns_setprio 0\nv_exp_f32 v28, v28\nv_exp_f32 v29, v29\n"
"v_exp_f32 v30, v30\nv_exp_f32 v31, v31\ns_waitcnt vmcnt(0) lgkmcnt(0)\ns_nop 0\nv_smfmac_f32_32x32x32_bf16 v[32:47], v[64:67], v[24:31], v52\n"
"s_setprio 0\nv_lshrrev_b32 v2, 4, v87\nv_bfe_u32 v120, v2, 2, 2\nv_bfe_u32 v3, v2, 4, 1\nv_cmp_eq_u32 vcc, v120, v3\nv_mov_b32 v3, 0x3f803f80\n"
"s_nop 0\nv_cndmask_b32 v120, 0, v3, vcc\nv_mov_b32 v121, v120\nv_mov_b32 v122, v120\nv_mov_b32 v123, v120\nv_mov_b32 v113, s40\nv_mov_b32 v115, s40\n"
"v_mov_b32 v117, s40\nv_mov_b32 v119, s40\ns_mov_b32 s3, 0\nv_add_f32 v0, |v32|, |v33|\nv_add_f32 v1, |v34|, |v35|\nv_add_f32 v2, |v36|, |v37|\n"
"v_add_f32 v3, |v38|, |v39|\nv_add_f32 v0, v0, v1\nv_add_f32 v2, v2, v3\nv_mul_f32 v1, 0x4d800000, v40\nv_add_f32 v0, v0, v2\nv_add_f32 v0, v0, v1\n"
"v_cmp_ngt_f32 vcc, 0x7f000000, v0\nv_mov_b32 v1, 0x12800000\nv_cmp_ngt_f32 s[48:49], v40, v1\ns_nop 0\ns_or_b64 vcc, vcc, s[48:49]\n"
"s_cbranch_vccnz .Lfb\ns_nop 1\n.Lfb_ret:\nv_rcp_f32 v0, v40\ns_nop 0\nv_mul_f32 v32, v0, v32\nv_mul_f32 v33, v0, v33\nv_mul_f32 v34, v0, v34\n"
"v_mul_f32 v35, v0, v35\nv_mul_f32 v36, v0, v36\nv_mul_f32 v37, v0, v37\nv_mul_f32 v38, v0, v38\nv_mul_f32 v39, v0, v39\n"
"v_cvt_pk_bf16_f32 v112, v32, v33\nv_cvt_pk_bf16_f32 v114, v36, v37\nbuffer_store_dwordx4 v[112:115], v87, s[12:15], s52 offen offset:0 sc1\n"
"v_cvt_pk_bf16_f32 v116, v34, v35\nv_cvt_pk_bf16_f32 v118, v38, v39\nbuffer_store_dwordx4 v[116:119], v87, s[12:15], s52 offen offset:1024 sc1\n"
"v_mfma_f32_16x16x32_bf16 v[124:127], v[120:123], v[128:131], 0\nv_mfma_f32_16x16x32_bf16 v[124:127], v[120:123], v[132:135], v[124:127]\n"
"v_mfma_f32_16x16x32_bf16 v[124:127], v[120:123], v[136:139], v[124:127]\nv_mfma_f32_16x16x32_bf16 v[124:127], v[120:123], v[140:143], v[124:127]\n"
"v_mfma_f32_16x16x32_bf16 v[124:127], v[120:123], v[144:147], v[124:127]\nv_mfma_f32_16x16x32_bf16 v[124:127], v[120:123], v[148:151], v[124:127]\n"
"v_mfma_f32_16x16x32_bf16 v[124:127], v[120:123], v[152:155], v[124:127]\nv_mfma_f32_16x16x32_bf16 v[124:127], v[120:123], v[156:159], v[124:127]\n"
"v_mfma_f32_16x16x32_bf16 v[124:127], v[120:123], v[160:163], v[124:127]\nv_mfma_f32_16x16x32_bf16 v[124:127], v[120:123], v[164:167], v[124:127]\n"
"v_mfma_f32_16x16x32_bf16 v[124:127], v[120:123], v[168:171], v[124:127]\nv_mfma_f32_16x16x32_bf16 v[124:127], v[120:123], v[172:175], v[124:127]\n"
"v_mfma_f32_16x16x32_bf16 v[124:127], v[120:123], v[176:179], v[124:127]\nv_mfma_f32_16x16x32_bf16 v[124:127], v[120:123], v[180:183], v[124:127]\n"
"v_mfma_f32_16x16x32_bf16 v[124:127], v[120:123], v[184:187], v[124:127]\nv_mfma_f32_16x16x32_bf16 v[124:127], v[120:123], v[188:191], v[124:127]\n"
"v_mfma_f32_16x16x32_bf16 v[124:127], v[120:123], v[192:195], v[124:127]\nv_mfma_f32_16x16x32_bf16 v[124:127], v[120:123], v[196:199], v[124:127]\n"
"v_mfma_f32_16x16x32_bf16 v[124:127], v[120:123], v[200:203], v[124:127]\nv_mfma_f32_16x16x32_bf16 v[124:127], v[120:123], v[204:207], v[124:127]\n"
"v_mfma_f32_16x16x32_bf16 v[124:127], v[120:123], v[208:211], v[124:127]\nv_mfma_f32_16x16x32_bf16 v[124:127], v[120:123], v[212:215], v[124:127]\n"
"v_mfma_f32_16x16x32_bf16 v[124:127], v[120:123], v[216:219], v[124:127]\nv_mfma_f32_16x16x32_bf16 v[124:127], v[120:123], v[220:223], v[124:127]\n"
"v_mfma_f32_16x16x32_bf16 v[124:127], v[120:123], v[224:227], v[124:127]\nv_mfma_f32_16x16x32_bf16 v[124:127], v[120:123], v[228:231], v[124:127]\n"
"v_mfma_f32_16x16x32_bf16 v[124:127], v[120:123], v[232:235], v[124:127]\nv_mfma_f32_16x16x32_bf16 v[124:127], v[120:123], v[236:239], v[124:127]\n"
"v_mfma_f32_16x16x32_bf16 v[124:127], v[120:123], v[240:243], v[124:127]\nv_mfma_f32_16x16x32_bf16 v[124:127], v[120:123], v[244:247], v[124:127]\n"
"v_mfma_f32_16x16x32_bf16 v[124:127], v[120:123], v[248:251], v[124:127]\nv_mfma_f32_16x16x32_bf16 v[124:127], v[120:123], v[252:255], v[124:127]\n"
"s_nop 7\nv_cvt_pk_bf16_f32 v125, v124, v124\nv_and_b32 v125, 0xffff0000, v125\nv_sub_f32 v126, v124, v125\nv_cvt_pk_bf16_f32 v84, v124, v126\n"
"v_and_b32 v125, 0xffff0000, v84\nv_sub_f32 v126, v126, v125\nv_cvt_pk_bf16_f32 v85, v126, 0\nv_lshl_or_b32 v85, v86, 16, v85\n"
"v_add_u32 v86, s61, v87\nv_lshlrev_b32 v0, 16, v80\nv_and_b32 v1, 0xffff0000, v80\nv_lshlrev_b32 v2, 16, v81\nv_and_b32 v3, 0xffff0000, v81\n"
"v_lshlrev_b32 v4, 16, v82\nv_and_b32 v5, 0xffff0000, v82\nv_lshlrev_b32 v6, 16, v83\nv_and_b32 v7, 0xffff0000, v83\nv_mov_b32 v8, 0\nv_mov_b32 v9, 0\n"
"v_mov_b32 v10, 0\nv_mov_b32 v11, 0\nv_mov_b32 v12, 0\nv_mov_b32 v13, 0\nv_mov_b32 v14, 0\nv_mov_b32 v15, 0\ns_lshl_b32 s47, s57, 16\n"
"s_add_u32 s47, s47, 0x8000\ns_xor_b32 s48, s56, 0\ns_mul_i32 s48, s48, 6144\ns_add_u32 s52, s48, s47\ns_xor_b32 s48, s56, 1\n"
"s_mul_i32 s48, s48, 6144\ns_add_u32 s53, s48, s47\ns_xor_b32 s48, s56, 2\ns_mul_i32 s48, s48, 6144\ns_add_u32 s54, s48, s47\ns_xor_b32 s48, s56, 3\n"
"s_mul_i32 s48, s48, 6144\ns_add_u32 s55, s48, s47\ns_waitcnt lgkmcnt(0)\ns_barrier\n"
"buffer_load_dwordx4 v[32:35], v87, s[12:15], s20 offen offset:0 sc1\nbuffer_load_dwordx4 v[16:19], v87, s[12:15], s20 offen offset:1024 sc1\n"
"buffer_load_dwordx4 v[36:39], v87, s[12:15], s20 offen offset:2048 sc1\nbuffer_load_dwordx4 v[20:23], v87, s[12:15], s20 offen offset:3072 sc1\n"
"buffer_load_dwordx4 v[40:43], v87, s[12:15], s21 offen offset:0 sc1\nbuffer_load_dwordx4 v[24:27], v87, s[12:15], s21 offen offset:1024 sc1\n"
"buffer_load_dwordx4 v[44:47], v87, s[12:15], s21 offen offset:2048 sc1\nbuffer_load_dwordx4 v[28:31], v87, s[12:15], s21 offen offset:3072 sc1\n"
"buffer_load_dwordx4 v[48:51], v87, s[12:15], s22 offen offset:0 sc1\nbuffer_load_dwordx4 v[64:67], v87, s[12:15], s22 offen offset:1024 sc1\n"
"buffer_load_dwordx4 v[52:55], v87, s[12:15], s22 offen offset:2048 sc1\nbuffer_load_dwordx4 v[68:71], v87, s[12:15], s22 offen offset:3072 sc1\n"
"buffer_load_dwordx4 v[56:59], v87, s[12:15], s23 offen offset:0 sc1\nbuffer_load_dwordx4 v[72:75], v87, s[12:15], s23 offen offset:1024 sc1\n"
"buffer_load_dwordx4 v[60:63], v87, s[12:15], s23 offen offset:2048 sc1\nbuffer_load_dwordx4 v[76:79], v87, s[12:15], s23 offen offset:3072 sc1\n"
"ds_read_b128 v[112:115], v86 offset:1024\nds_read_b128 v[116:119], v86 offset:0\nds_read_b128 v[120:123], v86 offset:3072\n"
"ds_read_b128 v[124:127], v86 offset:2048\ns_movk_i32 s3, 0xf0\ns_movk_i32 s47, 0xf0\ns_mov_b32 s48, 0\ns_getpc_b64 s[0:1]\n.Lobase:\n"
"s_waitcnt vmcnt(14)\nv_bitop3_b32 v17, v17, v19, s40 bitop3:0x7e\nv_bitop3_b32 v19, v33, v35, s40 bitop3:0x7e\nv_or_b32 v17, v17, v19\n"
"v_cmp_ne_u32 vcc, 0, v17\nv_mov_b32 v33, v16\nv_mov_b32 v35, v18\ns_cmp_lg_u64 vcc, 0\ns_cselect_b32 s49, 1, 0\ns_or_b32 s3, s3, s49\n"
"s_waitcnt vmcnt(12)\nv_bitop3_b32 v21, v21, v23, s40 bitop3:0x7e\nv_bitop3_b32 v23, v37, v39, s40 bitop3:0x7e\nv_or_b32 v21, v21, v23\n"
"v_cmp_ne_u32 vcc, 0, v21\nv_mov_b32 v37, v20\nv_mov_b32 v39, v22\ns_cmp_lg_u64 vcc, 0\ns_cselect_b32 s49, 2, 0\ns_or_b32 s3, s3, s49\n"
"s_waitcnt vmcnt(10)\nv_bitop3_b32 v25, v25, v27, s40 bitop3:0x7e\nv_bitop3_b32 v27, v41, v43, s40 bitop3:0x7e\nv_or_b32 v25, v25, v27\n"
"v_cmp_ne_u32 vcc, 0, v25\nv_mov_b32 v41, v24\nv_mov_b32 v43, v26\ns_cmp_lg_u64 vcc, 0\ns_cselect_b32 s49, 4, 0\ns_or_b32 s3, s3, s49\n"
"s_waitcnt vmcnt(8)\nv_bitop3_b32 v29, v29, v31, s40 bitop3:0x7e\nv_bitop3_b32 v31, v45, v47, s40 bitop3:0x7e\nv_or_b32 v29, v29, v31\n"
"v_cmp_ne_u32 vcc, 0, v29\nv_mov_b32 v45, v28\nv_mov_b32 v47, v30\ns_cmp_lg_u64 vcc, 0\ns_cselect_b32 s49, 8, 0\ns_or_b32 s3, s3, s49\n"
"s_bitcmp1_b32 s3, 0\ns_cbranch_scc1 .Lox_0\n.Los_0:\ns_waitcnt lgkmcnt(3)\nv_mfma_f32_32x32x16_bf16 v[16:31], v[112:115], v[32:35], 0\n"
"ds_read_b128 v[112:115], v86 offset:5120\ns_waitcnt lgkmcnt(3)\nv_mfma_f32_32x32x16_bf16 v[0:15], v[116:119], v[32:35], v[0:15]\n"
"ds_read_b128 v[116:119], v86 offset:4096\ns_bitcmp1_b32 s3, 1\ns_cbranch_scc1 .Lox_1\n.Los_1:\ns_waitcnt lgkmcnt(3)\n"
"v_mfma_f32_32x32x16_bf16 v[16:31], v[120:123], v[36:39], v[16:31]\nds_read_b128 v[120:123], v86 offset:7168\ns_waitcnt lgkmcnt(3)\n"
"v_mfma_f32_32x32x16_bf16 v[0:15], v[124:127], v[36:39], v[0:15]\nds_read_b128 v[124:127], v86 offset:6144\ns_bitcmp1_b32 s3, 2\n"
"s_cbranch_scc1 .Lox_2\n.Los_2:\ns_waitcnt lgkmcnt(3)\nv_mfma_f32_32x32x16_bf16 v[16:31], v[112:115], v[40:43], v[16:31]\n"
"ds_read_b128 v[112:115], v86 offset:9216\ns_waitcnt lgkmcnt(3)\nv_mfma_f32_32x32x16_bf16 v[0:15], v[116:119], v[40:43], v[0:15]\n"
"ds_read_b128 v[116:119], v86 offset:8192\ns_bitcmp1_b32 s3, 3\ns_cbranch_scc1 .Lox_3\n.Los_3:\ns_waitcnt lgkmcnt(3)\n"
"v_mfma_f32_32x32x16_bf16 v[16:31], v[120:123], v[44:47], v[16:31]\nds_read_b128 v[120:123], v86 offset:11264\ns_waitcnt lgkmcnt(3)\n"
"v_mfma_f32_32x32x16_bf16 v[0:15], v[124:127], v[44:47], v[0:15]\nds_read_b128 v[124:127], v86 offset:10240\ns_bitcmp1_b32 s3, 4\n"
"s_cbranch_scc0 .Los_4\n.Lox_4:\ns_bitcmp1_b32 s47, 4\ns_cbranch_scc0 .Lof_4\ns_waitcnt vmcnt(6)\nv_bitop3_b32 v65, v65, v67, s40 bitop3:0x7e\n"
"v_bitop3_b32 v67, v49, v51, s40 bitop3:0x7e\nv_or_b32 v65, v65, v67\nv_cmp_ne_u32 vcc, 0, v65\nv_mov_b32 v49, v64\nv_mov_b32 v51, v66\n"
"s_cbranch_vccnz .Lof_4\ns_bitset0_b32 s3, 4\n.Los_4:\ns_waitcnt lgkmcnt(3)\nv_mfma_f32_32x32x16_bf16 v[16:31], v[112:115], v[48:51], v[16:31]\n"
"ds_read_b128 v[112:115], v86 offset:13312\ns_waitcnt lgkmcnt(3)\nv_mfma_f32_32x32x16_bf16 v[0:15], v[116:119], v[48:51], v[0:15]\n"
"ds_read_b128 v[116:119], v86 offset:12288\ns_bitcmp1_b32 s3, 5\ns_cbranch_scc0 .Los_5\n.Lox_5:\ns_bitcmp1_b32 s47, 5\ns_cbranch_scc0 .Lof_5\n"
"s_waitcnt vmcnt(4)\nv_bitop3_b32 v69, v69, v71, s40 bitop3:0x7e\nv_bitop3_b32 v71, v53, v55, s40 bitop3:0x7e\nv_or_b32 v69, v69, v71\n"
"v_cmp_ne_u32 vcc, 0, v69\nv_mov_b32 v53, v68\nv_mov_b32 v55, v70\ns_cbranch_vccnz .Lof_5\ns_bitset0_b32 s3, 5\n.Los_5:\ns_waitcnt lgkmcnt(3)\n"
"v_mfma_f32_32x32x16_bf16 v[16:31], v[120:123], v[52:55], v[16:31]\nds_read_b128 v[120:123], v86 offset:15360\ns_waitcnt lgkmcnt(3)\n"
"v_mfma_f32_32x32x16_bf16 v[0:15], v[124:127], v[52:55], v[0:15]\nds_read_b128 v[124:127], v86 offset:14336\ns_bitcmp1_b32 s3, 6\n"
"s_cbranch_scc0 .Los_6\n.Lox_6:\ns_bitcmp1_b32 s47, 6\ns_cbranch_scc0 .Lof_6\ns_waitcnt vmcnt(2)\nv_bitop3_b32 v73, v73, v75, s40 bitop3:0x7e\n"
"v_bitop3_b32 v75, v57, v59, s40 bitop3:0x7e\nv_or_b32 v73, v73, v75\nv_cmp_ne_u32 vcc, 0, v73\nv_mov_b32 v57, v72\nv_mov_b32 v59, v74\n"
"s_cbranch_vccnz .Lof_6\ns_bitset0_b32 s3, 6\n.Los_6:\ns_waitcnt lgkmcnt(3)\nv_mfma_f32_32x32x16_bf16 v[16:31], v[112:115], v[56:59], v[16:31]\n"
"s_waitcnt lgkmcnt(2)\nv_mfma_f32_32x32x16_bf16 v[0:15], v[116:119], v[56:59], v[0:15]\ns_bitcmp1_b32 s3, 7\ns_cbranch_scc0 .Los_7\n.Lox_7:\n"
"s_bitcmp1_b32 s47, 7\ns_cbranch_scc0 .Lof_7\ns_waitcnt vmcnt(0)\nv_bitop3_b32 v77, v77, v79, s40 bitop3:0x7e\n"
"v_bitop3_b32 v79, v61, v63, s40 bitop3:0x7e\nv_or_b32 v77, v77, v79\nv_cmp_ne_u32 vcc, 0, v77\nv_mov_b32 v61, v76\nv_mov_b32 v63, v78\n"
"s_cbranch_vccnz .Lof_7\ns_bitset0_b32 s3, 7\n.Los_7:\ns_waitcnt lgkmcnt(1)\nv_mfma_f32_32x32x16_bf16 v[16:31], v[120:123], v[60:63], v[16:31]\n"
"s_waitcnt lgkmcnt(0)\nv_mfma_f32_32x32x16_bf16 v[0:15], v[124:127], v[60:63], v[0:15]\ns_nop 1\n.Lort_r:\nv_add_u32 v64, s52, v87\n"
"v_add_u32 v65, s53, v87\nv_add_u32 v66, s54, v87\nv_add_u32 v67, s55, v87\ns_mov_b32 s52, s28\ns_mov_b32 s51, s29\ns_lshl_b32 s50, s42, 7\n"
"s_add_u32 s50, s50, s51\ns_lshl_b32 s47, s44, 7\ns_add_u32 s51, s51, s47\nds_write_b128 v66, v[16:19] offset:2048\n"
"ds_write_b128 v66, v[20:23] offset:3072\nds_write_b128 v67, v[24:27] offset:4096\nds_write_b128 v67, v[28:31] offset:5120\n"
"ds_write_b128 v65, v[8:11]\nds_write_b128 v65, v[12:15] offset:1024\ns_add_u32 s101, s59, 0x4000\n"
"buffer_load_dwordx4 v[32:35], v87, s[12:15], s101 offen offset:1024 sc1\ns_add_u32 s101, s59, 0x4000\n"
"buffer_load_dwordx4 v[36:39], v87, s[12:15], s101 offen offset:2048 sc1\ns_add_u32 s101, s59, 0x4000\n"
"buffer_load_dwordx4 v[40:43], v87, s[12:15], s101 offen offset:3072 sc1\ns_add_u32 s101, s59, 0x5000\n"
"buffer_load_dwordx4 v[44:47], v87, s[12:15], s101 offen offset:0 sc1\ns_add_u32 s101, s59, 0x5000\n"
"buffer_load_dwordx4 v[48:51], v87, s[12:15], s101 offen offset:1024 sc1\ns_add_u32 s101, s59, 0x5000\n"
"buffer_load_dwordx4 v[52:55], v87, s[12:15], s101 offen offset:2048 sc1\ns_add_u32 s101, s59, 0x5000\n"
"buffer_load_dwordx4 v[56:59], v87, s[12:15], s101 offen offset:3072 sc1\ns_add_u32 s101, s59, 0x6000\n"
"buffer_load_dwordx4 v[60:63], v87, s[12:15], s101 offen offset:0 sc1\ns_waitcnt lgkmcnt(0)\ns_barrier\nds_read_b128 v[8:11], v64 offset:0\n"
"ds_read_b128 v[12:15], v64 offset:1024\nds_read_b128 v[16:19], v64 offset:2048\nds_read_b128 v[20:23], v64 offset:3072\n"
"ds_read_b128 v[24:27], v64 offset:4096\nds_read_b128 v[28:31], v64 offset:5120\nv_mov_b32 v72, s50\nv_lshlrev_b32 v73, 3, v87\n"
"v_and_b32 v73, 0x3ff, v73\ns_waitcnt lgkmcnt(5)\nv_pk_add_f32 v[0:1], v[0:1], v[8:9]\nv_pk_add_f32 v[2:3], v[2:3], v[10:11]\ns_waitcnt lgkmcnt(4)\n"
"v_pk_add_f32 v[4:5], v[4:5], v[12:13]\nv_pk_add_f32 v[6:7], v[6:7], v[14:15]\ns_waitcnt lgkmcnt(3)\nv_pk_add_f32 v[0:1], v[0:1], v[16:17]\n"
"v_pk_add_f32 v[2:3], v[2:3], v[18:19]\ns_waitcnt lgkmcnt(2)\nv_pk_add_f32 v[4:5], v[4:5], v[20:21]\nv_pk_add_f32 v[6:7], v[6:7], v[22:23]\n"
"s_waitcnt lgkmcnt(1)\nv_pk_add_f32 v[0:1], v[0:1], v[24:25]\nv_pk_add_f32 v[2:3], v[2:3], v[26:27]\ns_waitcnt lgkmcnt(0)\n"
"v_pk_add_f32 v[4:5], v[4:5], v[28:29]\nv_pk_add_f32 v[6:7], v[6:7], v[30:31]\nv_cvt_pk_bf16_f32 v80, v0, v1\nv_cvt_pk_bf16_f32 v81, v2, v3\n"
"v_cvt_pk_bf16_f32 v82, v4, v5\nv_cvt_pk_bf16_f32 v83, v6, v7\nbuffer_store_dwordx4 v[80:83], v87, s[12:15], s52 offen sc1\ns_lshl_b32 s47, s42, 12\n"
"s_mov_b32 s53, s30\ns_add_u32 s54, s53, 0x8000\nv_add_u32 v64, s47, v87\nv_add_u32 v65, 0x10000, v64\ns_lshl_b32 s47, s42, 2\n"
"s_add_u32 s47, s47, 0x20000\nv_lshl_add_u32 v66, v87, 1, s47\nv_add_u32 v86, 0x10000, v87\ns_mov_b32 s55, 0x3f803f80\nv_mov_b32 v68, 0\n"
"v_mov_b32 v69, 0\nv_mov_b32 v70, 0\nv_mov_b32 v71, 0\ns_waitcnt vmcnt(0)\nbuffer_load_dword v67, v73, s[12:15], s52 offen sc1\n"
"s_lshl_b32 s47, s57, 13\ns_add_u32 s61, s47, s61\nv_add_u32 v73, s61, v87\ns_barrier\nv_mov_b32 v74, s40\nv_mov_b32 v75, s41\ns_cmp_lg_u32 s42, 0\n"
"s_cbranch_scc1 .Lf2\ns_mov_b64 exec, 1\nbuffer_store_dwordx2 v[74:75], off, s[12:15], s51 sc1\ns_mov_b64 exec, -1\n.Lf2:\n"
"ds_write_b128 v73, v[32:35] offset:0\nds_write_b128 v73, v[36:39] offset:1024\nds_write_b128 v73, v[40:43] offset:2048\n"
"ds_write_b128 v73, v[44:47] offset:3072\nds_write_b128 v73, v[48:51] offset:4096\nds_write_b128 v73, v[52:55] offset:5120\n"
"ds_write_b128 v73, v[56:59] offset:6144\nds_write_b128 v73, v[60:63] offset:7168\ns_mov_b32 s47, 0\n.Lp2:\n"
"buffer_load_dwordx2 v[74:75], v72, s[12:15], 0 offen sc1\ns_waitcnt vmcnt(0)\nv_cmp_ne_u64 vcc, s[40:41], v[74:75]\ns_add_u32 s47, s47, 1\n"
"s_cmp_lt_u32 s47, 0x40000\ns_cselect_b64 vcc, vcc, 0\ns_cbranch_vccnz .Lp2\nbuffer_load_dwordx4 v[0:3], v87, s[12:15], s53 offen offset:0 sc1\n"
"buffer_load_dwordx4 v[4:7], v87, s[12:15], s53 offen offset:1024 sc1\nbuffer_load_dwordx4 v[8:11], v87, s[12:15], s53 offen offset:2048 sc1\n"
"buffer_load_dwordx4 v[12:15], v87, s[12:15], s53 offen offset:3072 sc1\nbuffer_load_dwordx4 v[16:19], v87, s[12:15], s54 offen offset:0 sc1\n"
"buffer_load_dwordx4 v[20:23], v87, s[12:15], s54 offen offset:1024 sc1\nbuffer_load_dwordx4 v[24:27], v87, s[12:15], s54 offen offset:2048 sc1\n"
"buffer_load_dwordx4 v[28:31], v87, s[12:15], s54 offen offset:3072 sc1\ns_waitcnt vmcnt(7)\nds_write_b128 v64, v[0:3] offset:32768\n"
"v_dot2c_f32_bf16 v68, s55, v0\nv_dot2c_f32_bf16 v69, v0, v0\nv_dot2c_f32_bf16 v68, s55, v1\nv_dot2c_f32_bf16 v69, v1, v1\n"
"v_dot2c_f32_bf16 v68, s55, v2\nv_dot2c_f32_bf16 v69, v2, v2\nv_dot2c_f32_bf16 v68, s55, v3\nv_dot2c_f32_bf16 v69, v3, v3\ns_waitcnt vmcnt(6)\n"
"ds_write_b128 v64, v[4:7] offset:33792\nv_dot2c_f32_bf16 v68, s55, v4\nv_dot2c_f32_bf16 v69, v4, v4\nv_dot2c_f32_bf16 v68, s55, v5\n"
"v_dot2c_f32_bf16 v69, v5, v5\nv_dot2c_f32_bf16 v68, s55, v6\nv_dot2c_f32_bf16 v69, v6, v6\nv_dot2c_f32_bf16 v68, s55, v7\n"
"v_dot2c_f32_bf16 v69, v7, v7\ns_waitcnt vmcnt(5)\nds_write_b128 v64, v[8:11] offset:34816\nv_dot2c_f32_bf16 v68, s55, v8\n"
"v_dot2c_f32_bf16 v69, v8, v8\nv_dot2c_f32_bf16 v68, s55, v9\nv_dot2c_f32_bf16 v69, v9, v9\nv_dot2c_f32_bf16 v68, s55, v10\n"
"v_dot2c_f32_bf16 v69, v10, v10\nv_dot2c_f32_bf16 v68, s55, v11\nv_dot2c_f32_bf16 v69, v11, v11\ns_waitcnt vmcnt(4)\n"
"ds_write_b128 v64, v[12:15] offset:35840\nv_dot2c_f32_bf16 v68, s55, v12\nv_dot2c_f32_bf16 v69, v12, v12\nv_dot2c_f32_bf16 v68, s55, v13\n"
"v_dot2c_f32_bf16 v69, v13, v13\nv_dot2c_f32_bf16 v68, s55, v14\nv_dot2c_f32_bf16 v69, v14, v14\nv_dot2c_f32_bf16 v68, s55, v15\n"
"v_dot2c_f32_bf16 v69, v15, v15\ns_nop 2\nv_permlane32_swap_b32 v68, v69\nv_add_f32 v68, v68, v69\nds_write_b32 v66, v68 offset:0\ns_waitcnt vmcnt(3)\n"
"ds_write_b128 v65, v[16:19] offset:32768\nv_dot2c_f32_bf16 v70, s55, v16\nv_dot2c_f32_bf16 v71, v16, v16\nv_dot2c_f32_bf16 v70, s55, v17\n"
"v_dot2c_f32_bf16 v71, v17, v17\nv_dot2c_f32_bf16 v70, s55, v18\nv_dot2c_f32_bf16 v71, v18, v18\nv_dot2c_f32_bf16 v70, s55, v19\n"
"v_dot2c_f32_bf16 v71, v19, v19\ns_waitcnt vmcnt(2)\nds_write_b128 v65, v[20:23] offset:33792\nv_dot2c_f32_bf16 v70, s55, v20\n"
"v_dot2c_f32_bf16 v71, v20, v20\nv_dot2c_f32_bf16 v70, s55, v21\nv_dot2c_f32_bf16 v71, v21, v21\nv_dot2c_f32_bf16 v70, s55, v22\n"
"v_dot2c_f32_bf16 v71, v22, v22\nv_dot2c_f32_bf16 v70, s55, v23\nv_dot2c_f32_bf16 v71, v23, v23\ns_waitcnt vmcnt(1)\n"
"ds_write_b128 v65, v[24:27] offset:34816\nv_dot2c_f32_bf16 v70, s55, v24\nv_dot2c_f32_bf16 v71, v24, v24\nv_dot2c_f32_bf16 v70, s55, v25\n"
"v_dot2c_f32_bf16 v71, v25, v25\nv_dot2c_f32_bf16 v70, s55, v26\nv_dot2c_f32_bf16 v71, v26, v26\nv_dot2c_f32_bf16 v70, s55, v27\n"
"v_dot2c_f32_bf16 v71, v27, v27\ns_waitcnt vmcnt(0)\nds_write_b128 v65, v[28:31] offset:35840\nv_dot2c_f32_bf16 v70, s55, v28\n"
"v_dot2c_f32_bf16 v71, v28, v28\nv_dot2c_f32_bf16 v70, s55, v29\nv_dot2c_f32_bf16 v71, v29, v29\nv_dot2c_f32_bf16 v70, s55, v30\n"
"v_dot2c_f32_bf16 v71, v30, v30\nv_dot2c_f32_bf16 v70, s55, v31\nv_dot2c_f32_bf16 v71, v31, v31\ns_nop 2\nv_permlane32_swap_b32 v70, v71\n"
"v_add_f32 v70, v70, v71\nds_write_b32 v66, v70 offset:2048\ns_lshl1_add_u32 s47, s44, s57\ns_lshl3_add_u32 s47, s47, s56\ns_lshl_b32 s47, s47, 11\n"
"s_add_u32 s48, s31, s47\ns_add_u32 s49, s48, 0x40000\ns_lshl_b32 s47, s42, 15\ns_add_u32 s52, s31, s47\ns_add_u32 s50, s50, 0x10000\n"
"s_add_u32 s51, s51, 0x10000\ns_waitcnt lgkmcnt(0)\ns_barrier\ns_cmp_eq_u32 s42, 7\ns_cbranch_scc1 .Lw7\ns_cmp_lt_u32 s42, 4\ns_setprio 2\n"
"s_cbranch_scc0 .Lpr0\ns_setprio 3\n.Lpr0:\nv_lshl_add_u32 v60, v87, 1, 0\nv_add_u32 v60, 0x20000, v60\nds_read_b128 v[64:67], v87 offset:32768\n"
"ds_read_b128 v[68:71], v87 offset:33792\nds_read_b128 v[72:75], v87 offset:34816\nds_read_b128 v[16:19], v60\nds_read_b128 v[20:23], v60 offset:16\n"
"ds_read_b128 v[76:79], v87 offset:35840\ns_waitcnt lgkmcnt(5)\nv_mfma_f32_32x32x16_bf16 v[0:15], v[128:131], v[64:67], 0\n"
"ds_read_b128 v[64:67], v87 offset:36864\ns_waitcnt lgkmcnt(5)\nv_mfma_f32_32x32x16_bf16 v[0:15], v[132:135], v[68:71], v[0:15]\n"
"ds_read_b128 v[68:71], v87 offset:37888\ns_waitcnt lgkmcnt(5)\nv_mfma_f32_32x32x16_bf16 v[0:15], v[136:139], v[72:75], v[0:15]\ns_waitcnt lgkmcnt(4)\n"
"v_add_f32 v16, v16, v17\nv_add_f32 v18, v18, v19\ns_waitcnt lgkmcnt(3)\nv_add_f32 v20, v20, v21\nds_read_b128 v[72:75], v87 offset:38912\n"
"s_waitcnt lgkmcnt(3)\nv_mfma_f32_32x32x16_bf16 v[0:15], v[140:143], v[76:79], v[0:15]\nv_add_f32 v22, v22, v23\nv_add_f32 v16, v16, v18\n"
"v_add_f32 v20, v20, v22\nds_read_b128 v[76:79], v87 offset:39936\ns_waitcnt lgkmcnt(3)\n"
"v_mfma_f32_32x32x16_bf16 v[0:15], v[144:147], v[64:67], v[0:15]\nv_add_f32 v21, v16, v20\nv_add_f32 v20, v16, v20\ns_nop 0\n"
"ds_read_b128 v[64:67], v87 offset:40960\ns_waitcnt lgkmcnt(3)\nv_mfma_f32_32x32x16_bf16 v[0:15], v[148:151], v[68:71], v[0:15]\n"
"v_permlane32_swap_b32 v21, v20\nv_mul_f32 v21, 0x3b000000, v21\nv_mul_f32 v20, 0x3b000000, v20\nds_read_b128 v[68:71], v87 offset:41984\n"
"s_waitcnt lgkmcnt(3)\nv_mfma_f32_32x32x16_bf16 v[0:15], v[152:155], v[72:75], v[0:15]\nv_fma_f32 v20, -v21, v21, v20\nv_max_f32 v20, 0, v20\n"
"v_cvt_pk_bf16_f32 v20, v20, v20\nds_read_b128 v[72:75], v87 offset:43008\ns_waitcnt lgkmcnt(3)\n"
"v_mfma_f32_32x32x16_bf16 v[0:15], v[156:159], v[76:79], v[0:15]\nv_lshlrev_b32 v20, 16, v20\nv_add_f32 v20, 0x3727c5ac, v20\n"
"v_cvt_pk_bf16_f32 v20, v20, v20\nds_read_b128 v[76:79], v87 offset:44032\ns_waitcnt lgkmcnt(3)\n"
"v_mfma_f32_32x32x16_bf16 v[0:15], v[160:163], v[64:67], v[0:15]\nv_lshlrev_b32 v20, 16, v20\nv_sqrt_f32 v20, v20\nv_cvt_pk_bf16_f32 v16, -v21, -v21\n"
"ds_read_b128 v[64:67], v87 offset:45056\ns_waitcnt lgkmcnt(3)\nv_mfma_f32_32x32x16_bf16 v[0:15], v[164:167], v[68:71], v[0:15]\n"
"v_cvt_pk_bf16_f32 v17, -v21, v20\nv_and_b32 v20, 0xffff0000, v17\nv_rcp_f32 v21, v20\nds_read_b128 v[68:71], v87 offset:46080\ns_waitcnt lgkmcnt(3)\n"
"v_mfma_f32_32x32x16_bf16 v[0:15], v[168:171], v[72:75], v[0:15]\ns_nop 0\nv_mul_f32 v22, v21, v21\nv_mul_f32 v19, 0xc0135761, v21\n"
"s_add_u32 s101, s59, 0x1000\nbuffer_load_dwordx4 v[112:115], v87, s[12:15], s101 offen offset:2048 sc1\nds_read_b128 v[72:75], v87 offset:47104\n"
"s_waitcnt lgkmcnt(3)\nv_mfma_f32_32x32x16_bf16 v[0:15], v[172:175], v[76:79], v[0:15]\nv_mul_f32 v21, 0xbdd2d3e8, v21\nv_mul_f32 v18, v21, v22\n"
"s_add_u32 s101, s59, 0x1000\nbuffer_load_dwordx4 v[116:119], v87, s[12:15], s101 offen offset:3072 sc1\nds_read_b128 v[76:79], v87 offset:48128\n"
"s_waitcnt lgkmcnt(3)\nv_mfma_f32_32x32x16_bf16 v[0:15], v[176:179], v[64:67], v[0:15]\ns_add_u32 s101, s59, 0x2000\n"
"buffer_load_dwordx4 v[120:123], v87, s[12:15], s101 offen offset:0 sc1\nds_read_b128 v[64:67], v87 offset:49152\ns_waitcnt lgkmcnt(3)\n"
"v_mfma_f32_32x32x16_bf16 v[0:15], v[180:183], v[68:71], v[0:15]\nv_mfma_f32_32x32x8_bf16 v[0:15], v[84:85], v[16:17], v[0:15]\n"
"s_add_u32 s101, s59, 0x2000\nbuffer_load_dwordx4 v[124:127], v87, s[12:15], s101 offen offset:1024 sc1\nds_read_b128 v[68:71], v87 offset:50176\n"
"s_waitcnt lgkmcnt(3)\nv_mfma_f32_32x32x16_bf16 v[0:15], v[184:187], v[72:75], v[0:15]\ns_add_u32 s101, s59, 0x2000\n"
"buffer_load_dwordx4 v[32:35], v87, s[12:15], s101 offen offset:2048 sc1\nds_read_b128 v[72:75], v87 offset:51200\ns_waitcnt lgkmcnt(3)\n"
"v_mfma_f32_32x32x16_bf16 v[0:15], v[188:191], v[76:79], v[0:15]\ns_add_u32 s101, s59, 0x2000\n"
"buffer_load_dwordx4 v[36:39], v87, s[12:15], s101 offen offset:3072 sc1\nds_read_b128 v[76:79], v87 offset:52224\ns_waitcnt lgkmcnt(3)\n"
"v_mfma_f32_32x32x16_bf16 v[0:15], v[192:195], v[64:67], v[0:15]\ns_add_u32 s101, s59, 0x3000\n"
"buffer_load_dwordx4 v[40:43], v87, s[12:15], s101 offen offset:0 sc1\nds_read_b128 v[64:67], v87 offset:53248\ns_waitcnt lgkmcnt(3)\n"
"v_mfma_f32_32x32x16_bf16 v[0:15], v[196:199], v[68:71], v[0:15]\ns_add_u32 s101, s59, 0x3000\n"
"buffer_load_dwordx4 v[44:47], v87, s[12:15], s101 offen offset:1024 sc1\nds_read_b128 v[68:71], v87 offset:54272\ns_waitcnt lgkmcnt(3)\n"
"v_mfma_f32_32x32x16_bf16 v[0:15], v[200:203], v[72:75], v[0:15]\ns_add_u32 s101, s59, 0x3000\n"
"buffer_load_dwordx4 v[48:51], v87, s[12:15], s101 offen offset:2048 sc1\nds_read_b128 v[72:75], v87 offset:55296\ns_waitcnt lgkmcnt(3)\n"
"v_mfma_f32_32x32x16_bf16 v[0:15], v[204:207], v[76:79], v[0:15]\ns_add_u32 s101, s59, 0x3000\n"
"buffer_load_dwordx4 v[52:55], v87, s[12:15], s101 offen offset:3072 sc1\nds_read_b128 v[76:79], v87 offset:56320\ns_waitcnt lgkmcnt(3)\n"
"v_mfma_f32_32x32x16_bf16 v[0:15], v[208:211], v[64:67], v[0:15]\ns_add_u32 s101, s59, 0x4000\n"
"buffer_load_dwordx4 v[56:59], v87, s[12:15], s101 offen offset:0 sc1\nds_read_b128 v[64:67], v87 offset:57344\ns_waitcnt lgkmcnt(3)\n"
"v_mfma_f32_32x32x16_bf16 v[0:15], v[212:215], v[68:71], v[0:15]\nds_read_b128 v[68:71], v87 offset:58368\ns_waitcnt lgkmcnt(3)\n"
"v_mfma_f32_32x32x16_bf16 v[0:15], v[216:219], v[72:75], v[0:15]\nds_read_b128 v[72:75], v87 offset:59392\ns_waitcnt lgkmcnt(3)\n"
"v_mfma_f32_32x32x16_bf16 v[0:15], v[220:223], v[76:79], v[0:15]\nds_read_b128 v[76:79], v87 offset:60416\ns_waitcnt lgkmcnt(3)\n"
"v_mfma_f32_32x32x16_bf16 v[0:15], v[224:227], v[64:67], v[0:15]\nds_read_b128 v[64:67], v87 offset:61440\ns_waitcnt lgkmcnt(3)\n"
"v_mfma_f32_32x32x16_bf16 v[0:15], v[228:231], v[68:71], v[0:15]\nds_read_b128 v[68:71], v87 offset:62464\ns_waitcnt lgkmcnt(3)\n"
"v_mfma_f32_32x32x16_bf16 v[0:15], v[232:235], v[72:75], v[0:15]\nds_read_b128 v[72:75], v87 offset:63488\ns_waitcnt lgkmcnt(3)\n"
"v_mfma_f32_32x32x16_bf16 v[0:15], v[236:239], v[76:79], v[0:15]\nds_read_b128 v[76:79], v87 offset:64512\ns_waitcnt lgkmcnt(3)\n"
"v_mfma_f32_32x32x16_bf16 v[0:15], v[240:243], v[64:67], v[0:15]\nds_read_b128 v[64:67], v86 offset:32768\ns_waitcnt lgkmcnt(3)\n"
"v_mfma_f32_32x32x16_bf16 v[0:15], v[244:247], v[68:71], v[0:15]\nds_read_b128 v[68:71], v86 offset:33792\ns_waitcnt lgkmcnt(3)\n"
"v_mfma_f32_32x32x16_bf16 v[0:15], v[248:251], v[72:75], v[0:15]\nds_read_b128 v[72:75], v86 offset:34816\ns_waitcnt lgkmcnt(3)\n"
"v_mfma_f32_32x32x16_bf16 v[0:15], v[252:255], v[76:79], v[0:15]\ns_cmp_lt_u32 s42, 4\ns_setprio 0\ns_cbranch_scc0 .Lpr1\ns_setprio 1\n.Lpr1:\n"
"s_nop 7\nv_mul_f32 v60, v0, v0\nv_mul_f32 v61, v1, v1\nv_mul_f32 v62, v2, v2\nv_mul_f32 v63, v3, v3\nv_fma_f32 v60, v60, v18, v19\n"
"v_fma_f32 v61, v61, v18, v19\nv_fma_f32 v62, v62, v18, v19\nv_fma_f32 v63, v63, v18, v19\nv_mul_f32 v60, v60, v0\nv_mul_f32 v61, v61, v1\n"
"v_mul_f32 v62, v62, v2\nv_mul_f32 v63, v63, v3\nv_exp_f32 v60, v60\nv_exp_f32 v61, v61\nv_exp_f32 v62, v62\nv_exp_f32 v63, v63\n"
"v_fma_f32 v60, v60, v20, v20\nv_fma_f32 v61, v61, v20, v20\nv_fma_f32 v62, v62, v20, v20\nv_fma_f32 v63, v63, v20, v20\nv_rcp_f32 v60, v60\n"
"v_rcp_f32 v61, v61\nv_rcp_f32 v62, v62\nv_rcp_f32 v63, v63\nv_mul_f32 v60, v60, v0\nv_mul_f32 v61, v61, v1\nv_mul_f32 v62, v62, v2\n"
"v_mul_f32 v63, v63, v3\nv_cvt_pk_bf16_f32 v0, v60, v61\nv_cvt_pk_bf16_f32 v1, v62, v63\nv_mul_f32 v60, v4, v4\nv_mul_f32 v61, v5, v5\n"
"v_mul_f32 v62, v6, v6\nv_mul_f32 v63, v7, v7\nv_fma_f32 v60, v60, v18, v19\nv_fma_f32 v61, v61, v18, v19\nv_fma_f32 v62, v62, v18, v19\n"
"v_fma_f32 v63, v63, v18, v19\nv_mul_f32 v60, v60, v4\nv_mul_f32 v61, v61, v5\nv_mul_f32 v62, v62, v6\nv_mul_f32 v63, v63, v7\nv_exp_f32 v60, v60\n"
"v_exp_f32 v61, v61\nv_exp_f32 v62, v62\nv_exp_f32 v63, v63\nv_fma_f32 v60, v60, v20, v20\nv_fma_f32 v61, v61, v20, v20\nv_fma_f32 v62, v62, v20, v20\n"
"v_fma_f32 v63, v63, v20, v20\nv_rcp_f32 v60, v60\nv_rcp_f32 v61, v61\nv_rcp_f32 v62, v62\nv_rcp_f32 v63, v63\nv_mul_f32 v60, v60, v4\n"
"v_mul_f32 v61, v61, v5\nv_mul_f32 v62, v62, v6\nv_mul_f32 v63, v63, v7\nv_cvt_pk_bf16_f32 v2, v60, v61\nv_cvt_pk_bf16_f32 v3, v62, v63\n"
"buffer_store_dwordx4 v[0:3], v87, s[12:15], s48 offen offset:0 sc1\nv_mul_f32 v60, v8, v8\nv_mul_f32 v61, v9, v9\nv_mul_f32 v62, v10, v10\n"
"v_mul_f32 v63, v11, v11\nv_fma_f32 v60, v60, v18, v19\nv_fma_f32 v61, v61, v18, v19\nv_fma_f32 v62, v62, v18, v19\nv_fma_f32 v63, v63, v18, v19\n"
"v_mul_f32 v60, v60, v8\nv_mul_f32 v61, v61, v9\nv_mul_f32 v62, v62, v10\nv_mul_f32 v63, v63, v11\nv_exp_f32 v60, v60\nv_exp_f32 v61, v61\n"
"v_exp_f32 v62, v62\nv_exp_f32 v63, v63\nv_fma_f32 v60, v60, v20, v20\nv_fma_f32 v61, v61, v20, v20\nv_fma_f32 v62, v62, v20, v20\n"
"v_fma_f32 v63, v63, v20, v20\nv_rcp_f32 v60, v60\nv_rcp_f32 v61, v61\nv_rcp_f32 v62, v62\nv_rcp_f32 v63, v63\nv_mul_f32 v60, v60, v8\n"
"v_mul_f32 v61, v61, v9\nv_mul_f32 v62, v62, v10\nv_mul_f32 v63, v63, v11\nv_cvt_pk_bf16_f32 v4, v60, v61\nv_cvt_pk_bf16_f32 v5, v62, v63\n"
"v_mul_f32 v60, v12, v12\nv_mul_f32 v61, v13, v13\nv_mul_f32 v62, v14, v14\nv_mul_f32 v63, v15, v15\nv_fma_f32 v60, v60, v18, v19\n"
"v_fma_f32 v61, v61, v18, v19\nv_fma_f32 v62, v62, v18, v19\nv_fma_f32 v63, v63, v18, v19\nv_mul_f32 v60, v60, v12\nv_mul_f32 v61, v61, v13\n"
"v_mul_f32 v62, v62, v14\nv_mul_f32 v63, v63, v15\nv_exp_f32 v60, v60\nv_exp_f32 v61, v61\nv_exp_f32 v62, v62\nv_exp_f32 v63, v63\n"
"v_fma_f32 v60, v60, v20, v20\nv_fma_f32 v61, v61, v20, v20\nv_fma_f32 v62, v62, v20, v20\nv_fma_f32 v63, v63, v20, v20\nv_rcp_f32 v60, v60\n"
"v_rcp_f32 v61, v61\nv_rcp_f32 v62, v62\nv_rcp_f32 v63, v63\nv_mul_f32 v60, v60, v12\nv_mul_f32 v61, v61, v13\nv_mul_f32 v62, v62, v14\n"
"v_mul_f32 v63, v63, v15\nv_cvt_pk_bf16_f32 v6, v60, v61\nv_cvt_pk_bf16_f32 v7, v62, v63\n"
"buffer_store_dwordx4 v[4:7], v87, s[12:15], s48 offen offset:1024 sc1\nv_lshl_add_u32 v60, v87, 1, 0\nv_add_u32 v60, 0x20000, v60\n"
"ds_read_b128 v[8:11], v60 offset:2048\nds_read_b128 v[12:15], v60 offset:2064\ns_cmp_lt_u32 s42, 4\ns_setprio 2\ns_cbranch_scc0 .Lpr2\ns_setprio 3\n"
".Lpr2:\nds_read_b128 v[76:79], v86 offset:35840\ns_waitcnt lgkmcnt(5)\nv_mfma_f32_32x32x16_bf16 v[16:31], v[128:131], v[64:67], 0\n"
"ds_read_b128 v[64:67], v86 offset:36864\ns_waitcnt lgkmcnt(5)\nv_mfma_f32_32x32x16_bf16 v[16:31], v[132:135], v[68:71], v[16:31]\n"
"v_add_u32 v1, s61, v87\nds_read_b128 v[68:71], v86 offset:37888\ns_waitcnt lgkmcnt(5)\n"
"v_mfma_f32_32x32x16_bf16 v[16:31], v[136:139], v[72:75], v[16:31]\ns_waitcnt lgkmcnt(4)\nv_add_f32 v8, v8, v9\nv_add_f32 v10, v10, v11\n"
"s_waitcnt lgkmcnt(3)\nv_add_f32 v12, v12, v13\nds_read_b128 v[72:75], v86 offset:38912\ns_waitcnt lgkmcnt(3)\n"
"v_mfma_f32_32x32x16_bf16 v[16:31], v[140:143], v[76:79], v[16:31]\nv_add_f32 v14, v14, v15\nv_add_f32 v8, v8, v10\nv_add_f32 v12, v12, v14\n"
"ds_read_b128 v[76:79], v86 offset:39936\ns_waitcnt lgkmcnt(3)\nv_mfma_f32_32x32x16_bf16 v[16:31], v[144:147], v[64:67], v[16:31]\n"
"v_add_f32 v13, v8, v12\nv_add_f32 v12, v8, v12\ns_nop 0\nds_read_b128 v[64:67], v86 offset:40960\ns_waitcnt lgkmcnt(3)\n"
"v_mfma_f32_32x32x16_bf16 v[16:31], v[148:151], v[68:71], v[16:31]\nv_permlane32_swap_b32 v13, v12\nv_mul_f32 v13, 0x3b000000, v13\n"
"v_mul_f32 v12, 0x3b000000, v12\nds_read_b128 v[68:71], v86 offset:41984\ns_waitcnt lgkmcnt(3)\n"
"v_mfma_f32_32x32x16_bf16 v[16:31], v[152:155], v[72:75], v[16:31]\nv_fma_f32 v12, -v13, v13, v12\nv_max_f32 v12, 0, v12\n"
"v_cvt_pk_bf16_f32 v12, v12, v12\nds_read_b128 v[72:75], v86 offset:43008\ns_waitcnt lgkmcnt(3)\n"
"v_mfma_f32_32x32x16_bf16 v[16:31], v[156:159], v[76:79], v[16:31]\nv_lshlrev_b32 v12, 16, v12\nv_add_f32 v12, 0x3727c5ac, v12\n"
"v_cvt_pk_bf16_f32 v12, v12, v12\nds_read_b128 v[76:79], v86 offset:44032\ns_waitcnt lgkmcnt(3)\n"
"v_mfma_f32_32x32x16_bf16 v[16:31], v[160:163], v[64:67], v[16:31]\nv_lshlrev_b32 v12, 16, v12\nv_sqrt_f32 v12, v12\nv_cvt_pk_bf16_f32 v8, -v13, -v13\n"
"ds_read_b128 v[64:67], v86 offset:45056\ns_waitcnt lgkmcnt(3)\nv_mfma_f32_32x32x16_bf16 v[16:31], v[164:167], v[68:71], v[16:31]\n"
"v_cvt_pk_bf16_f32 v9, -v13, v12\nv_and_b32 v12, 0xffff0000, v9\nv_rcp_f32 v13, v12\nds_read_b128 v[68:71], v86 offset:46080\ns_waitcnt lgkmcnt(3)\n"
"v_mfma_f32_32x32x16_bf16 v[16:31], v[168:171], v[72:75], v[16:31]\ns_nop 0\nv_mul_f32 v14, v13, v13\nv_mul_f32 v11, 0xc0135761, v13\n"
"ds_read_b128 v[72:75], v86 offset:47104\ns_waitcnt lgkmcnt(3)\nv_mfma_f32_32x32x16_bf16 v[16:31], v[172:175], v[76:79], v[16:31]\n"
"v_mul_f32 v13, 0xbdd2d3e8, v13\nv_mul_f32 v10, v13, v14\nds_read_b128 v[76:79], v86 offset:48128\ns_waitcnt lgkmcnt(3)\n"
"v_mfma_f32_32x32x16_bf16 v[16:31], v[176:179], v[64:67], v[16:31]\nds_read_b128 v[64:67], v86 offset:49152\ns_waitcnt lgkmcnt(3)\n"
"v_mfma_f32_32x32x16_bf16 v[16:31], v[180:183], v[68:71], v[16:31]\nv_mfma_f32_32x32x8_bf16 v[16:31], v[84:85], v[8:9], v[16:31]\n"
"ds_read_b128 v[68:71], v86 offset:50176\ns_waitcnt lgkmcnt(3)\nv_mfma_f32_32x32x16_bf16 v[16:31], v[184:187], v[72:75], v[16:31]\n"
"ds_read_b128 v[72:75], v86 offset:51200\ns_waitcnt lgkmcnt(3)\nv_mfma_f32_32x32x16_bf16 v[16:31], v[188:191], v[76:79], v[16:31]\n"
"ds_read_b128 v[76:79], v86 offset:52224\ns_waitcnt lgkmcnt(3)\nv_mfma_f32_32x32x16_bf16 v[16:31], v[192:195], v[64:67], v[16:31]\n"
"ds_read_b128 v[64:67], v86 offset:53248\ns_waitcnt lgkmcnt(3)\nv_mfma_f32_32x32x16_bf16 v[16:31], v[196:199], v[68:71], v[16:31]\n"
"ds_read_b128 v[68:71], v86 offset:54272\ns_waitcnt lgkmcnt(3)\nv_mfma_f32_32x32x16_bf16 v[16:31], v[200:203], v[72:75], v[16:31]\ns_waitcnt vmcnt(0)\n"
"v_mov_b32 v60, 0x27000\nv_mov_b32 v61, 1\ns_mov_b64 exec, 1\nds_add_rtn_u32 v61, v60, v61 offset:0\ns_mov_b64 exec, -1\n"
"ds_read_b128 v[72:75], v86 offset:55296\ns_waitcnt lgkmcnt(4)\nv_mfma_f32_32x32x16_bf16 v[16:31], v[204:207], v[76:79], v[16:31]\n"
"ds_read_b128 v[76:79], v86 offset:56320\ns_waitcnt lgkmcnt(4)\nv_mfma_f32_32x32x16_bf16 v[16:31], v[208:211], v[64:67], v[16:31]\n"
"ds_read_b128 v[64:67], v86 offset:57344\ns_waitcnt lgkmcnt(4)\nv_mfma_f32_32x32x16_bf16 v[16:31], v[212:215], v[68:71], v[16:31]\n"
"s_waitcnt lgkmcnt(3)\nv_readfirstlane_b32 s47, v61\ns_nop 0\ns_cmp_lg_u32 s47, 6\ns_cbranch_scc1 .Lg0\nv_mov_b32 v60, s40\nv_mov_b32 v61, s41\n"
"s_mov_b64 exec, 1\nbuffer_store_dwordx2 v[60:61], off, s[12:15], s51 sc1\ns_mov_b64 exec, -1\n.Lg0:\nds_read_b128 v[68:71], v86 offset:58368\n"
"s_waitcnt lgkmcnt(3)\nv_mfma_f32_32x32x16_bf16 v[16:31], v[216:219], v[72:75], v[16:31]\nds_read_b128 v[128:131], v1 offset:0\n"
"ds_read_b128 v[72:75], v86 offset:59392\ns_waitcnt lgkmcnt(4)\nv_mfma_f32_32x32x16_bf16 v[16:31], v[220:223], v[76:79], v[16:31]\n"
"ds_read_b128 v[132:135], v1 offset:1024\nds_read_b128 v[76:79], v86 offset:60416\ns_waitcnt lgkmcnt(5)\n"
"v_mfma_f32_32x32x16_bf16 v[16:31], v[224:227], v[64:67], v[16:31]\nds_read_b128 v[136:139], v1 offset:2048\nds_read_b128 v[64:67], v86 offset:61440\n"
"s_waitcnt lgkmcnt(6)\nv_mfma_f32_32x32x16_bf16 v[16:31], v[228:231], v[68:71], v[16:31]\nds_read_b128 v[140:143], v1 offset:3072\n"
"ds_read_b128 v[68:71], v86 offset:62464\ns_waitcnt lgkmcnt(6)\nv_mfma_f32_32x32x16_bf16 v[16:31], v[232:235], v[72:75], v[16:31]\n"
"ds_read_b128 v[144:147], v1 offset:4096\nv_lshrrev_b32 v223, 9, v87\nv_lshlrev_b32 v223, 3, v223\ns_lshl_b32 s47, s63, 5\n"
"buffer_load_dwordx2 v[216:217], v223, s[36:39], s47 offen\nbuffer_load_dwordx2 v[218:219], v223, s[36:39], s47 offen offset:16\n"
"ds_read_b128 v[72:75], v86 offset:63488\ns_waitcnt lgkmcnt(6)\nv_mfma_f32_32x32x16_bf16 v[16:31], v[236:239], v[76:79], v[16:31]\n"
"ds_read_b128 v[148:151], v1 offset:5120\nds_read_b128 v[76:79], v86 offset:64512\ns_waitcnt lgkmcnt(6)\n"
"v_mfma_f32_32x32x16_bf16 v[16:31], v[240:243], v[64:67], v[16:31]\nds_read_b128 v[152:155], v1 offset:6144\ns_waitcnt lgkmcnt(5)\n"
"v_mfma_f32_32x32x16_bf16 v[16:31], v[244:247], v[68:71], v[16:31]\nds_read_b128 v[156:159], v1 offset:7168\ns_waitcnt lgkmcnt(4)\n"
"v_mfma_f32_32x32x16_bf16 v[16:31], v[248:251], v[72:75], v[16:31]\ns_waitcnt lgkmcnt(2)\n"
"v_mfma_f32_32x32x16_bf16 v[16:31], v[252:255], v[76:79], v[16:31]\ns_mul_i32 s47, s42, 3072\ns_add_u32 s47, s47, 0x21000\nv_add_u32 v0, s47, v87\n"
"ds_read_b128 v[64:67], v0 offset:0\nds_read_b128 v[68:71], v0 offset:1024\nds_read_b128 v[72:75], v0 offset:2048\ns_setprio 0\ns_nop 4\n"
"v_mul_f32 v60, v16, v16\nv_mul_f32 v61, v17, v17\nv_mul_f32 v62, v18, v18\nv_mul_f32 v63, v19, v19\nv_fma_f32 v60, v60, v10, v11\n"
"v_fma_f32 v61, v61, v10, v11\nv_fma_f32 v62, v62, v10, v11\nv_fma_f32 v63, v63, v10, v11\nv_mul_f32 v60, v60, v16\nv_mul_f32 v61, v61, v17\n"
"v_mul_f32 v62, v62, v18\nv_mul_f32 v63, v63, v19\nv_exp_f32 v60, v60\nv_exp_f32 v61, v61\nv_exp_f32 v62, v62\nv_exp_f32 v63, v63\n"
"v_fma_f32 v60, v60, v12, v12\nv_fma_f32 v61, v61, v12, v12\nv_fma_f32 v62, v62, v12, v12\nv_fma_f32 v63, v63, v12, v12\nv_rcp_f32 v60, v60\n"
"v_rcp_f32 v61, v61\nv_rcp_f32 v62, v62\nv_rcp_f32 v63, v63\nv_mul_f32 v60, v60, v16\nv_mul_f32 v61, v61, v17\nv_mul_f32 v62, v62, v18\n"
"v_mul_f32 v63, v63, v19\nv_cvt_pk_bf16_f32 v16, v60, v61\nv_cvt_pk_bf16_f32 v17, v62, v63\nv_mul_f32 v60, v20, v20\nv_mul_f32 v61, v21, v21\n"
"v_mul_f32 v62, v22, v22\nv_mul_f32 v63, v23, v23\nv_fma_f32 v60, v60, v10, v11\nv_fma_f32 v61, v61, v10, v11\nv_fma_f32 v62, v62, v10, v11\n"
"v_fma_f32 v63, v63, v10, v11\nv_mul_f32 v60, v60, v20\nv_mul_f32 v61, v61, v21\nv_mul_f32 v62, v62, v22\nv_mul_f32 v63, v63, v23\nv_exp_f32 v60, v60\n"
"v_exp_f32 v61, v61\nv_exp_f32 v62, v62\nv_exp_f32 v63, v63\nv_fma_f32 v60, v60, v12, v12\nv_fma_f32 v61, v61, v12, v12\nv_fma_f32 v62, v62, v12, v12\n"
"v_fma_f32 v63, v63, v12, v12\nv_rcp_f32 v60, v60\nv_rcp_f32 v61, v61\nv_rcp_f32 v62, v62\nv_rcp_f32 v63, v63\nv_mul_f32 v60, v60, v20\n"
"v_mul_f32 v61, v61, v21\nv_mul_f32 v62, v62, v22\nv_mul_f32 v63, v63, v23\nv_cvt_pk_bf16_f32 v18, v60, v61\nv_cvt_pk_bf16_f32 v19, v62, v63\n"
"buffer_store_dwordx4 v[16:19], v87, s[12:15], s49 offen offset:0 sc1\nv_mul_f32 v60, v24, v24\nv_mul_f32 v61, v25, v25\nv_mul_f32 v62, v26, v26\n"
"v_mul_f32 v63, v27, v27\nv_fma_f32 v60, v60, v10, v11\nv_fma_f32 v61, v61, v10, v11\nv_fma_f32 v62, v62, v10, v11\nv_fma_f32 v63, v63, v10, v11\n"
"v_mul_f32 v60, v60, v24\nv_mul_f32 v61, v61, v25\nv_mul_f32 v62, v62, v26\nv_mul_f32 v63, v63, v27\nv_exp_f32 v60, v60\nv_exp_f32 v61, v61\n"
"v_exp_f32 v62, v62\nv_exp_f32 v63, v63\nv_fma_f32 v60, v60, v12, v12\nv_fma_f32 v61, v61, v12, v12\nv_fma_f32 v62, v62, v12, v12\n"
"v_fma_f32 v63, v63, v12, v12\nv_rcp_f32 v60, v60\nv_rcp_f32 v61, v61\nv_rcp_f32 v62, v62\nv_rcp_f32 v63, v63\nv_mul_f32 v60, v60, v24\n"
"v_mul_f32 v61, v61, v25\nv_mul_f32 v62, v62, v26\nv_mul_f32 v63, v63, v27\nv_cvt_pk_bf16_f32 v20, v60, v61\nv_cvt_pk_bf16_f32 v21, v62, v63\n"
"v_mul_f32 v60, v28, v28\nv_mul_f32 v61, v29, v29\nv_mul_f32 v62, v30, v30\nv_mul_f32 v63, v31, v31\nv_fma_f32 v60, v60, v10, v11\n"
"v_fma_f32 v61, v61, v10, v11\nv_fma_f32 v62, v62, v10, v11\nv_fma_f32 v63, v63, v10, v11\nv_mul_f32 v60, v60, v28\nv_mul_f32 v61, v61, v29\n"
"v_mul_f32 v62, v62, v30\nv_mul_f32 v63, v63, v31\nv_exp_f32 v60, v60\nv_exp_f32 v61, v61\nv_exp_f32 v62, v62\nv_exp_f32 v63, v63\n"
"v_fma_f32 v60, v60, v12, v12\nv_fma_f32 v61, v61, v12, v12\nv_fma_f32 v62, v62, v12, v12\nv_fma_f32 v63, v63, v12, v12\nv_rcp_f32 v60, v60\n"
"v_rcp_f32 v61, v61\nv_rcp_f32 v62, v62\nv_rcp_f32 v63, v63\nv_mul_f32 v60, v60, v28\nv_mul_f32 v61, v61, v29\nv_mul_f32 v62, v62, v30\n"
"v_mul_f32 v63, v63, v31\nv_cvt_pk_bf16_f32 v22, v60, v61\nv_cvt_pk_bf16_f32 v23, v62, v63\n"
"buffer_store_dwordx4 v[20:23], v87, s[12:15], s49 offen offset:1024 sc1\nv_mov_b32 v78, s50\n"
"buffer_load_dwordx2 v[76:77], v78, s[12:15], 0 offen sc1\ns_add_u32 s51, s51, 0x10000\ns_waitcnt vmcnt(0)\nv_mov_b32 v60, 0x27000\nv_mov_b32 v61, 1\n"
"s_mov_b64 exec, 1\nds_add_rtn_u32 v61, v60, v61 offset:4\ns_mov_b64 exec, -1\nv_cmp_ne_u64 vcc, s[40:41], v[76:77]\ns_waitcnt lgkmcnt(0)\n"
"v_readfirstlane_b32 s47, v61\ns_nop 0\ns_cmp_lg_u32 s47, 6\ns_cbranch_scc1 .Lg1\nv_mov_b32 v60, s40\nv_mov_b32 v61, s41\ns_mov_b64 exec, 1\n"
"buffer_store_dwordx2 v[60:61], off, s[12:15], s51 sc1\ns_mov_b64 exec, -1\n.Lg1:\ns_branch .Lffn2\n.Lw7:\ns_cmp_lg_u32 s2, 0\ns_cbranch_scc1 .Lnoe7\n"
"s_xor_b32 s49, s41, 0x85ebca6b\ns_sub_u32 s48, s40, 0x9e3779b9\ns_add_u32 s48, s48, 1\ns_addc_u32 s49, s49, 0\ns_mov_b32 s47, 0xa60000\n"
"v_mov_b32 v0, s48\nv_mov_b32 v1, s49\ns_mov_b64 exec, 1\nbuffer_store_dwordx2 v[0:1], off, s[12:15], s47 sc1\ns_mov_b64 exec, -1\ns_nop 1\n.Lnoe7:\n"
"s_add_u32 s101, s59, 0x1000\nbuffer_load_dwordx4 v[112:115], v87, s[12:15], s101 offen offset:2048 sc1\ns_add_u32 s101, s59, 0x1000\n"
"buffer_load_dwordx4 v[116:119], v87, s[12:15], s101 offen offset:3072 sc1\ns_add_u32 s101, s59, 0x2000\n"
"buffer_load_dwordx4 v[120:123], v87, s[12:15], s101 offen offset:0 sc1\ns_add_u32 s101, s59, 0x2000\n"
"buffer_load_dwordx4 v[124:127], v87, s[12:15], s101 offen offset:1024 sc1\ns_add_u32 s101, s59, 0x2000\n"
"buffer_load_dwordx4 v[32:35], v87, s[12:15], s101 offen offset:2048 sc1\ns_add_u32 s101, s59, 0x2000\n"
"buffer_load_dwordx4 v[36:39], v87, s[12:15], s101 offen offset:3072 sc1\ns_add_u32 s101, s59, 0x3000\n"
"buffer_load_dwordx4 v[40:43], v87, s[12:15], s101 offen offset:0 sc1\ns_add_u32 s101, s59, 0x3000\n"
"buffer_load_dwordx4 v[44:47], v87, s[12:15], s101 offen offset:1024 sc1\ns_add_u32 s101, s59, 0x3000\n"
"buffer_load_dwordx4 v[48:51], v87, s[12:15], s101 offen offset:2048 sc1\ns_add_u32 s101, s59, 0x3000\n"
"buffer_load_dwordx4 v[52:55], v87, s[12:15], s101 offen offset:3072 sc1\ns_add_u32 s101, s59, 0x4000\n"
"buffer_load_dwordx4 v[56:59], v87, s[12:15], s101 offen offset:0 sc1\nv_add_u32 v1, s61, v87\nds_read_b128 v[128:131], v1 offset:0\n"
"ds_read_b128 v[132:135], v1 offset:1024\nds_read_b128 v[136:139], v1 offset:2048\nds_read_b128 v[140:143], v1 offset:3072\n"
"ds_read_b128 v[144:147], v1 offset:4096\nds_read_b128 v[148:151], v1 offset:5120\nds_read_b128 v[152:155], v1 offset:6144\n"
"ds_read_b128 v[156:159], v1 offset:7168\nv_lshrrev_b32 v223, 9, v87\nv_lshlrev_b32 v223, 3, v223\ns_lshl_b32 s47, s63, 5\n"
"buffer_load_dwordx2 v[216:217], v223, s[36:39], s47 offen\nbuffer_load_dwordx2 v[218:219], v223, s[36:39], s47 offen offset:16\n"
"v_add_u32 v0, 0x26400, v87\nds_read_b128 v[64:67], v0 offset:0\nds_read_b128 v[68:71], v0 offset:1024\nds_read_b128 v[72:75], v0 offset:2048\n"
"v_mov_b32 v78, s50\ns_mov_b64 vcc, -1\ns_nop 0\n.Lffn2:\ns_setprio 0\ns_waitcnt lgkmcnt(0)\ns_cbranch_vccz .Lp3_k\ns_mov_b32 s47, 0\n.Lp3:\n"
"buffer_load_dwordx2 v[76:77], v78, s[12:15], 0 offen sc1\ns_waitcnt vmcnt(0)\nv_cmp_ne_u64 vcc, s[40:41], v[76:77]\ns_add_u32 s47, s47, 1\n"
"s_cmp_lt_u32 s47, 0x40000\ns_cselect_b64 vcc, vcc, 0\ns_cbranch_vccnz .Lp3\n.Lp3_k:\ns_add_u32 s101, s52, 0x0\n"
"buffer_load_dwordx4 v[160:163], v87, s[12:15], s101 offen offset:0 sc1\ns_add_u32 s101, s52, 0x0\n"
"buffer_load_dwordx4 v[164:167], v87, s[12:15], s101 offen offset:1024 sc1\ns_add_u32 s101, s52, 0x0\n"
"buffer_load_dwordx4 v[168:171], v87, s[12:15], s101 offen offset:2048 sc1\ns_add_u32 s101, s52, 0x0\n"
"buffer_load_dwordx4 v[172:175], v87, s[12:15], s101 offen offset:3072 sc1\ns_add_u32 s101, s52, 0x1000\n"
"buffer_load_dwordx4 v[176:179], v87, s[12:15], s101 offen offset:0 sc1\ns_add_u32 s101, s52, 0x1000\n"
"buffer_load_dwordx4 v[180:183], v87, s[12:15], s101 offen offset:1024 sc1\ns_add_u32 s101, s52, 0x1000\n"
"buffer_load_dwordx4 v[184:187], v87, s[12:15], s101 offen offset:2048 sc1\ns_add_u32 s101, s52, 0x1000\n"
"buffer_load_dwordx4 v[188:191], v87, s[12:15], s101 offen offset:3072 sc1\ns_add_u32 s101, s52, 0x4000\n"
"buffer_load_dwordx4 v[192:195], v87, s[12:15], s101 offen offset:0 sc1\ns_add_u32 s101, s52, 0x4000\n"
"buffer_load_dwordx4 v[196:199], v87, s[12:15], s101 offen offset:1024 sc1\ns_add_u32 s101, s52, 0x4000\n"
"buffer_load_dwordx4 v[200:203], v87, s[12:15], s101 offen offset:2048 sc1\ns_add_u32 s101, s52, 0x4000\n"
"buffer_load_dwordx4 v[204:207], v87, s[12:15], s101 offen offset:3072 sc1\ns_add_u32 s101, s52, 0x5000\n"
"buffer_load_dwordx4 v[208:211], v87, s[12:15], s101 offen offset:0 sc1\ns_add_u32 s101, s52, 0x5000\n"
"buffer_load_dwordx4 v[212:215], v87, s[12:15], s101 offen offset:1024 sc1\nv_and_b32 v60, 0xf0, v87\nv_bfe_u32 v61, v87, 8, 2\n"
"s_lshl_b32 s47, s42, 6\nv_lshl_add_u32 v61, v61, 4, s47\nv_lshrrev_b32 v62, 1, v60\nv_lshl_add_u32 v79, v61, 6, v62\nv_xor_b32 v61, v61, v60\n"
"v_lshl_add_u32 v223, v60, 5, v61\nv_add_u32 v84, 0x10000, v223\ns_lshl_b32 s54, s43, 16\ns_lshl_b32 s47, s44, 7\ns_add_u32 s54, s54, s47\n"
"s_add_u32 s55, s54, 0x8000\ns_lshl1_add_u32 s47, s56, s57\ns_mul_i32 s47, s47, 0x420\ns_or_b32 s47, s47, s61\nv_lshrrev_b32 v60, 5, v87\n"
"v_and_b32 v60, 16, v60\nv_xor_b32 v60, v60, v87\nv_xor_b32 v86, s47, v60\nv_xor_b32 v60, 0x420, v86\nv_xor_b32 v61, 0x840, v86\n"
"v_xor_b32 v62, 0xc60, v86\nv_xor_b32 v63, 0x1080, v86\nv_xor_b32 v220, 0x14a0, v86\nv_xor_b32 v221, 0x18c0, v86\nv_xor_b32 v222, 0x1ce0, v86\n"
"s_sub_u32 s48, s57, 1\ns_not_b32 s49, s48\nv_mov_b32 v8, 0\nv_mov_b32 v232, 0\nv_mov_b32 v9, 0\nv_mov_b32 v233, 0\nv_mov_b32 v10, 0\n"
"v_mov_b32 v234, 0\nv_mov_b32 v11, 0\nv_mov_b32 v235, 0\nv_mov_b32 v12, 0\nv_mov_b32 v236, 0\nv_mov_b32 v13, 0\nv_mov_b32 v237, 0\nv_mov_b32 v14, 0\n"
"v_mov_b32 v238, 0\nv_mov_b32 v15, 0\nv_mov_b32 v239, 0\nv_lshlrev_b32 v0, 16, v80\nv_and_b32 v1, 0xffff0000, v80\nv_lshlrev_b32 v2, 16, v81\n"
"v_and_b32 v3, 0xffff0000, v81\nv_lshlrev_b32 v4, 16, v82\nv_and_b32 v5, 0xffff0000, v82\nv_lshlrev_b32 v6, 16, v83\nv_and_b32 v7, 0xffff0000, v83\n"
"v_lshlrev_b32 v16, 16, v216\nv_and_b32 v17, 0xffff0000, v216\nv_lshlrev_b32 v18, 16, v217\nv_and_b32 v19, 0xffff0000, v217\n"
"v_lshlrev_b32 v20, 16, v218\nv_and_b32 v21, 0xffff0000, v218\nv_lshlrev_b32 v22, 16, v219\nv_and_b32 v23, 0xffff0000, v219\nv_add_f32 v0, v0, v16\n"
"v_add_f32 v1, v1, v17\nv_add_f32 v2, v2, v18\nv_add_f32 v3, v3, v19\nv_add_f32 v4, v4, v20\nv_add_f32 v5, v5, v21\nv_add_f32 v6, v6, v22\n"
"v_add_f32 v7, v7, v23\nv_and_b32 v228, s49, v0\nv_and_b32 v229, s49, v1\nv_and_b32 v230, s49, v2\nv_and_b32 v231, s49, v3\nv_and_b32 v224, s49, v4\n"
"v_and_b32 v225, s49, v5\nv_and_b32 v226, s49, v6\nv_and_b32 v227, s49, v7\nv_and_b32 v0, s48, v0\nv_and_b32 v1, s48, v1\nv_and_b32 v2, s48, v2\n"
"v_and_b32 v3, s48, v3\nv_and_b32 v4, s48, v4\nv_and_b32 v5, s48, v5\nv_and_b32 v6, s48, v6\nv_and_b32 v7, s48, v7\ns_add_u32 s50, s50, 0x10000\n"
"v_mov_b32 v78, s50\ns_add_u32 s52, s52, 0x40000\ns_waitcnt vmcnt(13)\nv_mfma_f32_32x32x16_bf16 v[16:31], v[92:95], v[160:163], 0\n"
"v_mfma_f32_32x32x16_bf16 v[0:15], v[88:91], v[160:163], v[0:15]\ns_waitcnt vmcnt(12)\n"
"v_mfma_f32_32x32x16_bf16 v[16:31], v[100:103], v[164:167], v[16:31]\nv_mfma_f32_32x32x16_bf16 v[0:15], v[96:99], v[164:167], v[0:15]\n"
"s_waitcnt vmcnt(11)\nv_mfma_f32_32x32x16_bf16 v[16:31], v[108:111], v[168:171], v[16:31]\n"
"v_mfma_f32_32x32x16_bf16 v[0:15], v[104:107], v[168:171], v[0:15]\nbuffer_load_dwordx2 v[76:77], v78, s[12:15], 0 offen sc1\ns_waitcnt vmcnt(11)\n"
"v_mfma_f32_32x32x16_bf16 v[16:31], v[116:119], v[172:175], v[16:31]\nv_mfma_f32_32x32x16_bf16 v[0:15], v[112:115], v[172:175], v[0:15]\n"
"s_waitcnt vmcnt(10)\nv_mfma_f32_32x32x16_bf16 v[16:31], v[124:127], v[176:179], v[16:31]\n"
"v_mfma_f32_32x32x16_bf16 v[0:15], v[120:123], v[176:179], v[0:15]\ns_waitcnt vmcnt(9)\n"
"v_mfma_f32_32x32x16_bf16 v[16:31], v[36:39], v[180:183], v[16:31]\nv_mfma_f32_32x32x16_bf16 v[0:15], v[32:35], v[180:183], v[0:15]\n"
"s_waitcnt vmcnt(8)\nv_mfma_f32_32x32x16_bf16 v[16:31], v[44:47], v[184:187], v[16:31]\n"
"v_mfma_f32_32x32x16_bf16 v[0:15], v[40:43], v[184:187], v[0:15]\ns_waitcnt vmcnt(7)\n"
"v_mfma_f32_32x32x16_bf16 v[16:31], v[52:55], v[188:191], v[16:31]\nv_mfma_f32_32x32x16_bf16 v[0:15], v[48:51], v[188:191], v[0:15]\n"
"s_waitcnt vmcnt(6)\nv_mfma_f32_32x32x16_bf16 v[16:31], v[128:131], v[192:195], v[16:31]\n"
"v_mfma_f32_32x32x16_bf16 v[0:15], v[56:59], v[192:195], v[0:15]\ns_waitcnt vmcnt(5)\n"
"v_mfma_f32_32x32x16_bf16 v[16:31], v[136:139], v[196:199], v[16:31]\nv_mfma_f32_32x32x16_bf16 v[0:15], v[132:135], v[196:199], v[0:15]\n"
"s_waitcnt vmcnt(4)\nv_mfma_f32_32x32x16_bf16 v[16:31], v[144:147], v[200:203], v[16:31]\n"
"v_mfma_f32_32x32x16_bf16 v[0:15], v[140:143], v[200:203], v[0:15]\ns_waitcnt vmcnt(3)\n"
"v_mfma_f32_32x32x16_bf16 v[16:31], v[152:155], v[204:207], v[16:31]\nv_mfma_f32_32x32x16_bf16 v[0:15], v[148:151], v[204:207], v[0:15]\n"
"s_waitcnt vmcnt(2)\nv_mfma_f32_32x32x16_bf16 v[16:31], v[64:67], v[208:211], v[16:31]\n"
"v_mfma_f32_32x32x16_bf16 v[0:15], v[156:159], v[208:211], v[0:15]\ns_waitcnt vmcnt(1)\n"
"v_mfma_f32_32x32x16_bf16 v[16:31], v[72:75], v[212:215], v[16:31]\nv_mfma_f32_32x32x16_bf16 v[0:15], v[68:71], v[212:215], v[0:15]\ns_mov_b32 s47, 0\n"
"s_branch .Lp4_c\n.Lp4:\ns_waitcnt vmcnt(0)\nbuffer_load_dwordx2 v[76:77], v78, s[12:15], 0 offen sc1\n.Lp4_c:\ns_waitcnt vmcnt(0)\n"
"v_cmp_ne_u64 vcc, s[40:41], v[76:77]\ns_add_u32 s47, s47, 1\ns_cmp_lt_u32 s47, 0x40000\ns_cselect_b64 vcc, vcc, 0\ns_cbranch_vccnz .Lp4\n"
"s_add_u32 s101, s52, 0x0\nbuffer_load_dwordx4 v[160:163], v87, s[12:15], s101 offen offset:0 sc1\ns_add_u32 s101, s52, 0x0\n"
"buffer_load_dwordx4 v[164:167], v87, s[12:15], s101 offen offset:1024 sc1\ns_add_u32 s101, s52, 0x0\n"
"buffer_load_dwordx4 v[168:171], v87, s[12:15], s101 offen offset:2048 sc1\ns_add_u32 s101, s52, 0x0\n"
"buffer_load_dwordx4 v[172:175], v87, s[12:15], s101 offen offset:3072 sc1\ns_add_u32 s101, s52, 0x1000\n"
"buffer_load_dwordx4 v[176:179], v87, s[12:15], s101 offen offset:0 sc1\ns_add_u32 s101, s52, 0x1000\n"
"buffer_load_dwordx4 v[180:183], v87, s[12:15], s101 offen offset:1024 sc1\ns_add_u32 s101, s52, 0x1000\n"
"buffer_load_dwordx4 v[184:187], v87, s[12:15], s101 offen offset:2048 sc1\ns_add_u32 s101, s52, 0x1000\n"
"buffer_load_dwordx4 v[188:191], v87, s[12:15], s101 offen offset:3072 sc1\ns_add_u32 s101, s52, 0x4000\n"
"buffer_load_dwordx4 v[192:195], v87, s[12:15], s101 offen offset:0 sc1\ns_add_u32 s101, s52, 0x4000\n"
"buffer_load_dwordx4 v[196:199], v87, s[12:15], s101 offen offset:1024 sc1\ns_add_u32 s101, s52, 0x4000\n"
"buffer_load_dwordx4 v[200:203], v87, s[12:15], s101 offen offset:2048 sc1\ns_add_u32 s101, s52, 0x4000\n"
"buffer_load_dwordx4 v[204:207], v87, s[12:15], s101 offen offset:3072 sc1\ns_add_u32 s101, s52, 0x5000\n"
"buffer_load_dwordx4 v[208:211], v87, s[12:15], s101 offen offset:0 sc1\ns_add_u32 s101, s52, 0x5000\n"
"buffer_load_dwordx4 v[212:215], v87, s[12:15], s101 offen offset:1024 sc1\nds_write_b128 v63, v[16:19] offset:0\n"
"ds_write_b128 v220, v[20:23] offset:0\nds_write_b128 v221, v[24:27] offset:0\nds_write_b128 v222, v[28:31] offset:0\n"
"ds_write_b128 v86, v[0:3] offset:0\nds_write_b128 v60, v[4:7] offset:0\nds_write_b128 v61, v[8:11] offset:0\nds_write_b128 v62, v[12:15] offset:0\n"
"s_cmp_lt_u32 s42, 4\ns_cbranch_scc1 .Lfza\ns_waitcnt lgkmcnt(0)\ns_barrier\nds_read_b128 v[0:3], v223 offset:0\n"
"ds_read_b128 v[4:7], v223 offset:8192\nds_read_b128 v[8:11], v223 offset:16384\nds_read_b128 v[12:15], v223 offset:24576\n"
"ds_read_b128 v[16:19], v84 offset:0\nds_read_b128 v[20:23], v84 offset:8192\nds_read_b128 v[24:27], v84 offset:16384\n"
"ds_read_b128 v[28:31], v84 offset:24576\ns_waitcnt lgkmcnt(6)\nv_add_f32 v0, v0, v4\nv_add_f32 v1, v1, v5\nv_add_f32 v2, v2, v6\n"
"v_add_f32 v3, v3, v7\ns_waitcnt lgkmcnt(5)\nv_add_f32 v0, v0, v8\nv_add_f32 v1, v1, v9\nv_add_f32 v2, v2, v10\nv_add_f32 v3, v3, v11\n"
"s_waitcnt lgkmcnt(4)\nv_add_f32 v0, v0, v12\nv_add_f32 v1, v1, v13\nv_add_f32 v2, v2, v14\nv_add_f32 v3, v3, v15\ns_waitcnt lgkmcnt(3)\n"
"v_add_f32 v0, v0, v16\nv_add_f32 v1, v1, v17\nv_add_f32 v2, v2, v18\nv_add_f32 v3, v3, v19\ns_waitcnt lgkmcnt(2)\nv_add_f32 v0, v0, v20\n"
"v_add_f32 v1, v1, v21\nv_add_f32 v2, v2, v22\nv_add_f32 v3, v3, v23\ns_waitcnt lgkmcnt(1)\nv_add_f32 v0, v0, v24\nv_add_f32 v1, v1, v25\n"
"v_add_f32 v2, v2, v26\nv_add_f32 v3, v3, v27\ns_waitcnt lgkmcnt(0)\nv_add_f32 v0, v0, v28\nv_add_f32 v1, v1, v29\nv_add_f32 v2, v2, v30\n"
"v_add_f32 v3, v3, v31\nv_cvt_pk_bf16_f32 v0, v0, v1\nv_cvt_pk_bf16_f32 v1, v2, v3\ns_nop 1\n.Lfza:\ns_waitcnt vmcnt(13)\n"
"v_mfma_f32_32x32x16_bf16 v[240:255], v[92:95], v[160:163], 0\nv_mfma_f32_32x32x16_bf16 v[224:239], v[88:91], v[160:163], v[224:239]\n"
"s_waitcnt vmcnt(12)\nv_mfma_f32_32x32x16_bf16 v[240:255], v[100:103], v[164:167], v[240:255]\n"
"v_mfma_f32_32x32x16_bf16 v[224:239], v[96:99], v[164:167], v[224:239]\ns_waitcnt vmcnt(11)\n"
"v_mfma_f32_32x32x16_bf16 v[240:255], v[108:111], v[168:171], v[240:255]\nv_mfma_f32_32x32x16_bf16 v[224:239], v[104:107], v[168:171], v[224:239]\n"
"s_waitcnt vmcnt(10)\nv_mfma_f32_32x32x16_bf16 v[240:255], v[116:119], v[172:175], v[240:255]\n"
"v_mfma_f32_32x32x16_bf16 v[224:239], v[112:115], v[172:175], v[224:239]\ns_waitcnt vmcnt(9)\n"
"v_mfma_f32_32x32x16_bf16 v[240:255], v[124:127], v[176:179], v[240:255]\nv_mfma_f32_32x32x16_bf16 v[224:239], v[120:123], v[176:179], v[224:239]\n"
"s_waitcnt vmcnt(8)\nv_mfma_f32_32x32x16_bf16 v[240:255], v[36:39], v[180:183], v[240:255]\n"
"v_mfma_f32_32x32x16_bf16 v[224:239], v[32:35], v[180:183], v[224:239]\ns_waitcnt vmcnt(7)\n"
"v_mfma_f32_32x32x16_bf16 v[240:255], v[44:47], v[184:187], v[240:255]\nv_mfma_f32_32x32x16_bf16 v[224:239], v[40:43], v[184:187], v[224:239]\n"
"s_waitcnt vmcnt(6)\nv_mfma_f32_32x32x16_bf16 v[240:255], v[52:55], v[188:191], v[240:255]\n"
"v_mfma_f32_32x32x16_bf16 v[224:239], v[48:51], v[188:191], v[224:239]\ns_waitcnt vmcnt(5)\n"
"v_mfma_f32_32x32x16_bf16 v[240:255], v[128:131], v[192:195], v[240:255]\nv_mfma_f32_32x32x16_bf16 v[224:239], v[56:59], v[192:195], v[224:239]\n"
"s_waitcnt vmcnt(4)\nv_mfma_f32_32x32x16_bf16 v[240:255], v[136:139], v[196:199], v[240:255]\n"
"v_mfma_f32_32x32x16_bf16 v[224:239], v[132:135], v[196:199], v[224:239]\nv_mov_b32 v77, 0x27000\ns_waitcnt vmcnt(3)\n"
"v_mfma_f32_32x32x16_bf16 v[240:255], v[144:147], v[200:203], v[240:255]\nv_mfma_f32_32x32x16_bf16 v[224:239], v[140:143], v[200:203], v[224:239]\n"
"ds_read_b32 v76, v77 offset:4\ns_waitcnt vmcnt(2)\nv_mfma_f32_32x32x16_bf16 v[240:255], v[152:155], v[204:207], v[240:255]\n"
"v_mfma_f32_32x32x16_bf16 v[224:239], v[148:151], v[204:207], v[224:239]\ns_waitcnt vmcnt(1)\n"
"v_mfma_f32_32x32x16_bf16 v[240:255], v[64:67], v[208:211], v[240:255]\nv_mfma_f32_32x32x16_bf16 v[224:239], v[156:159], v[208:211], v[224:239]\n"
"s_waitcnt lgkmcnt(0)\nv_readfirstlane_b32 s47, v76\ns_waitcnt vmcnt(0)\nv_mfma_f32_32x32x16_bf16 v[240:255], v[72:75], v[212:215], v[240:255]\n"
"v_mfma_f32_32x32x16_bf16 v[224:239], v[68:71], v[212:215], v[224:239]\ns_cmp_lg_u32 s47, 7\ns_cbranch_scc1 .Lcw\n.Lcw_r:\ns_nop 8\n"
"ds_write_b128 v63, v[240:243] offset:32768\nds_write_b128 v220, v[244:247] offset:32768\nds_write_b128 v221, v[248:251] offset:32768\n"
"ds_write_b128 v222, v[252:255] offset:32768\nds_write_b128 v86, v[224:227] offset:32768\nds_write_b128 v60, v[228:231] offset:32768\n"
"ds_write_b128 v61, v[232:235] offset:32768\nds_write_b128 v62, v[236:239] offset:32768\ns_cmp_lt_u32 s42, 4\ns_cbranch_scc0 .Lfzb\n"
"s_waitcnt lgkmcnt(0)\ns_barrier\nds_read_b128 v[0:3], v223 offset:0\nds_read_b128 v[4:7], v223 offset:8192\nds_read_b128 v[8:11], v223 offset:16384\n"
"ds_read_b128 v[12:15], v223 offset:24576\nds_read_b128 v[16:19], v84 offset:0\nds_read_b128 v[20:23], v84 offset:8192\n"
"ds_read_b128 v[24:27], v84 offset:16384\nds_read_b128 v[28:31], v84 offset:24576\ns_waitcnt lgkmcnt(6)\nv_add_f32 v0, v0, v4\nv_add_f32 v1, v1, v5\n"
"v_add_f32 v2, v2, v6\nv_add_f32 v3, v3, v7\ns_waitcnt lgkmcnt(5)\nv_add_f32 v0, v0, v8\nv_add_f32 v1, v1, v9\nv_add_f32 v2, v2, v10\n"
"v_add_f32 v3, v3, v11\ns_waitcnt lgkmcnt(4)\nv_add_f32 v0, v0, v12\nv_add_f32 v1, v1, v13\nv_add_f32 v2, v2, v14\nv_add_f32 v3, v3, v15\n"
"s_waitcnt lgkmcnt(3)\nv_add_f32 v0, v0, v16\nv_add_f32 v1, v1, v17\nv_add_f32 v2, v2, v18\nv_add_f32 v3, v3, v19\ns_waitcnt lgkmcnt(2)\n"
"v_add_f32 v0, v0, v20\nv_add_f32 v1, v1, v21\nv_add_f32 v2, v2, v22\nv_add_f32 v3, v3, v23\ns_waitcnt lgkmcnt(1)\nv_add_f32 v0, v0, v24\n"
"v_add_f32 v1, v1, v25\nv_add_f32 v2, v2, v26\nv_add_f32 v3, v3, v27\ns_waitcnt lgkmcnt(0)\nv_add_f32 v0, v0, v28\nv_add_f32 v1, v1, v29\n"
"v_add_f32 v2, v2, v30\nv_add_f32 v3, v3, v31\nv_cvt_pk_bf16_f32 v0, v0, v1\nv_cvt_pk_bf16_f32 v1, v2, v3\ns_nop 1\n.Lfzb:\n"
"buffer_store_dwordx2 v[0:1], v79, s[16:19], s54 offen sc1\ns_waitcnt lgkmcnt(0)\ns_barrier\nds_read_b128 v[224:227], v223 offset:32768\n"
"ds_read_b128 v[228:231], v223 offset:40960\nds_read_b128 v[232:235], v223 offset:49152\nds_read_b128 v[236:239], v223 offset:57344\n"
"ds_read_b128 v[240:243], v84 offset:32768\nds_read_b128 v[244:247], v84 offset:40960\nds_read_b128 v[248:251], v84 offset:49152\n"
"ds_read_b128 v[252:255], v84 offset:57344\ns_waitcnt lgkmcnt(6)\nv_pk_add_f32 v[224:225], v[224:225], v[228:229]\n"
"v_pk_add_f32 v[226:227], v[226:227], v[230:231]\ns_waitcnt lgkmcnt(5)\nv_pk_add_f32 v[224:225], v[224:225], v[232:233]\n"
"v_pk_add_f32 v[226:227], v[226:227], v[234:235]\ns_waitcnt lgkmcnt(4)\nv_pk_add_f32 v[224:225], v[224:225], v[236:237]\n"
"v_pk_add_f32 v[226:227], v[226:227], v[238:239]\ns_waitcnt lgkmcnt(3)\nv_pk_add_f32 v[224:225], v[224:225], v[240:241]\n"
"v_pk_add_f32 v[226:227], v[226:227], v[242:243]\ns_waitcnt lgkmcnt(2)\nv_pk_add_f32 v[224:225], v[224:225], v[244:245]\n"
"v_pk_add_f32 v[226:227], v[226:227], v[246:247]\ns_waitcnt lgkmcnt(1)\nv_pk_add_f32 v[224:225], v[224:225], v[248:249]\n"
"v_pk_add_f32 v[226:227], v[226:227], v[250:251]\ns_waitcnt lgkmcnt(0)\nv_pk_add_f32 v[224:225], v[224:225], v[252:253]\n"
"v_pk_add_f32 v[226:227], v[226:227], v[254:255]\nv_cvt_pk_bf16_f32 v224, v224, v225\nv_cvt_pk_bf16_f32 v225, v226, v227\n"
"buffer_store_dwordx2 v[224:225], v79, s[16:19], s55 offen sc1\ns_nop 0\n.Lfin:\n.Lnoe:\ns_endpgm\n.Lfb:\ns_lshl_b32 s48, s43, 16\n"
"s_add_u32 s48, s48, 0x200000\ns_add_u32 s49, s48, 0x10000\ns_mov_b32 s53, s48\nv_mov_b32 v72, 0xff800000\ns_nop 1\n.Lfb1:\n"
"buffer_load_dwordx4 v[60:63], v87, s[12:15], s53 offen sc1\ns_waitcnt vmcnt(0)\nv_mfma_f32_32x32x16_bf16 v[0:15], v[60:63], v[68:71], 0\n"
"s_add_u32 s53, s53, 0x400\ns_nop 10\nv_max3_f32 v72, v72, v0, v1\nv_max3_f32 v72, v72, v2, v3\nv_max3_f32 v72, v72, v4, v5\n"
"v_max3_f32 v72, v72, v6, v7\nv_max3_f32 v72, v72, v8, v9\nv_max3_f32 v72, v72, v10, v11\nv_max3_f32 v72, v72, v12, v13\n"
"v_max3_f32 v72, v72, v14, v15\ns_cmp_lt_u32 s53, s49\ns_nop 0\ns_cbranch_scc1 .Lfb1\nv_mov_b32 v73, v72\ns_nop 1\nv_permlane32_swap_b32 v72, v73\n"
"v_max_f32 v72, v72, v73\nv_mov_b32 v32, 0\nv_mov_b32 v33, 0\nv_mov_b32 v34, 0\nv_mov_b32 v35, 0\nv_mov_b32 v36, 0\nv_mov_b32 v37, 0\n"
"v_mov_b32 v38, 0\nv_mov_b32 v39, 0\nv_mov_b32 v40, 0\nv_mov_b32 v41, 0\nv_mov_b32 v42, 0\nv_mov_b32 v43, 0\nv_mov_b32 v44, 0\nv_mov_b32 v45, 0\n"
"v_mov_b32 v46, 0\nv_mov_b32 v47, 0\nv_and_b32 v73, 0xf0, v87\nv_lshrrev_b32 v74, 9, v87\nv_lshl_add_u32 v73, v74, 8, v73\n"
"v_add_u32 v73, 0x200000, v73\nv_and_b32 v74, 0x100, v87\nv_cmp_ne_u32 vcc, 0, v74\nv_mov_b32 v74, 0x3f803f80\ns_mov_b32 s53, s48\ns_nop 2\n.Lfb2:\n"
"buffer_load_dwordx4 v[60:63], v87, s[12:15], s53 offen sc1\nbuffer_load_dwordx4 v[16:19], v73, s[12:15], s53 offen sc1\n"
"buffer_load_dwordx4 v[20:23], v73, s[12:15], s53 offen offset:512 sc1\ns_waitcnt vmcnt(2)\nv_mfma_f32_32x32x16_bf16 v[0:15], v[60:63], v[68:71], 0\n"
"s_add_u32 s53, s53, 0x400\ns_waitcnt vmcnt(1)\nv_cndmask_b32 v16, v16, v74, vcc\nv_cndmask_b32 v17, v17, v74, vcc\nv_cndmask_b32 v18, v18, v74, vcc\n"
"v_cndmask_b32 v19, v19, v74, vcc\ns_waitcnt vmcnt(0)\nv_cndmask_b32 v20, v20, v74, vcc\nv_cndmask_b32 v21, v21, v74, vcc\n"
"v_cndmask_b32 v22, v22, v74, vcc\nv_cndmask_b32 v23, v23, v74, vcc\ns_nop 0\nv_sub_f32 v0, v0, v72\nv_sub_f32 v1, v1, v72\nv_sub_f32 v2, v2, v72\n"
"v_sub_f32 v3, v3, v72\nv_sub_f32 v4, v4, v72\nv_sub_f32 v5, v5, v72\nv_sub_f32 v6, v6, v72\nv_sub_f32 v7, v7, v72\nv_sub_f32 v8, v8, v72\n"
"v_sub_f32 v9, v9, v72\nv_sub_f32 v10, v10, v72\nv_sub_f32 v11, v11, v72\nv_sub_f32 v12, v12, v72\nv_sub_f32 v13, v13, v72\nv_sub_f32 v14, v14, v72\n"
"v_sub_f32 v15, v15, v72\nv_exp_f32 v0, v0\nv_exp_f32 v1, v1\nv_exp_f32 v2, v2\nv_exp_f32 v3, v3\nv_exp_f32 v4, v4\nv_exp_f32 v5, v5\n"
"v_exp_f32 v6, v6\nv_exp_f32 v7, v7\nv_exp_f32 v8, v8\nv_exp_f32 v9, v9\nv_exp_f32 v10, v10\nv_exp_f32 v11, v11\nv_exp_f32 v12, v12\n"
"v_exp_f32 v13, v13\nv_exp_f32 v14, v14\nv_exp_f32 v15, v15\nv_smfmac_f32_32x32x32_bf16 v[32:47], v[16:19], v[0:7], v52\ns_nop 0\n"
"v_smfmac_f32_32x32x32_bf16 v[32:47], v[20:23], v[8:15], v52\ns_cmp_lt_u32 s53, s49\ns_nop 10\ns_cbranch_scc1 .Lfb2\ns_branch .Lfb_ret\n.Lcw:\n"
"s_mov_b32 s48, 0\n.Lcw1:\nds_read_b32 v76, v77 offset:4\ns_add_u32 s48, s48, 1\ns_waitcnt lgkmcnt(0)\nv_readfirstlane_b32 s47, v76\n"
"s_cmp_gt_u32 s48, 0x100000\ns_cbranch_scc1 .Lcw_r\ns_cmp_lg_u32 s47, 7\ns_cbranch_scc1 .Lcw1\ns_branch .Lcw_r\n.Lox_0:\ns_bitcmp1_b32 s47, 0\n"
"s_cbranch_scc0 .Lof_0\nv_bitop3_b32 v65, v65, v67, s40 bitop3:0x7e\nv_bitop3_b32 v67, v33, v35, s40 bitop3:0x7e\nv_or_b32 v65, v65, v67\n"
"v_cmp_ne_u32 vcc, 0, v65\nv_mov_b32 v33, v64\nv_mov_b32 v35, v66\ns_cbranch_vccnz .Lof_0\ns_bitset0_b32 s3, 0\ns_branch .Los_0\n.Lox_1:\n"
"s_bitcmp1_b32 s47, 1\ns_cbranch_scc0 .Lof_1\nv_bitop3_b32 v69, v69, v71, s40 bitop3:0x7e\nv_bitop3_b32 v71, v37, v39, s40 bitop3:0x7e\n"
"v_or_b32 v69, v69, v71\nv_cmp_ne_u32 vcc, 0, v69\nv_mov_b32 v37, v68\nv_mov_b32 v39, v70\ns_cbranch_vccnz .Lof_1\ns_bitset0_b32 s3, 1\n"
"s_branch .Los_1\n.Lox_2:\ns_bitcmp1_b32 s47, 2\ns_cbranch_scc0 .Lof_2\nv_bitop3_b32 v73, v73, v75, s40 bitop3:0x7e\n"
"v_bitop3_b32 v75, v41, v43, s40 bitop3:0x7e\nv_or_b32 v73, v73, v75\nv_cmp_ne_u32 vcc, 0, v73\nv_mov_b32 v41, v72\nv_mov_b32 v43, v74\n"
"s_cbranch_vccnz .Lof_2\ns_bitset0_b32 s3, 2\ns_branch .Los_2\n.Lox_3:\ns_bitcmp1_b32 s47, 3\ns_cbranch_scc0 .Lof_3\n"
"v_bitop3_b32 v77, v77, v79, s40 bitop3:0x7e\nv_bitop3_b32 v79, v45, v47, s40 bitop3:0x7e\nv_or_b32 v77, v77, v79\nv_cmp_ne_u32 vcc, 0, v77\n"
"v_mov_b32 v45, v76\nv_mov_b32 v47, v78\ns_cbranch_vccnz .Lof_3\ns_bitset0_b32 s3, 3\ns_branch .Los_3\n.Lof_7:\ns_add_u32 s24, s0, .Lox_7-.Lobase\n"
"s_addc_u32 s25, s1, 0\ns_waitcnt vmcnt(0)\ns_branch .Loc_8\n.Lof_6:\ns_add_u32 s24, s0, .Lox_6-.Lobase\ns_addc_u32 s25, s1, 0\ns_waitcnt vmcnt(0)\n"
"s_branch .Loc_7\n.Lof_5:\ns_add_u32 s24, s0, .Lox_5-.Lobase\ns_addc_u32 s25, s1, 0\ns_waitcnt vmcnt(0)\ns_branch .Loc_6\n.Lof_4:\n"
"s_add_u32 s24, s0, .Lox_4-.Lobase\ns_addc_u32 s25, s1, 0\ns_waitcnt vmcnt(0)\ns_branch .Loc_5\n.Lof_3:\ns_add_u32 s24, s0, .Lox_3-.Lobase\n"
"s_addc_u32 s25, s1, 0\ns_waitcnt vmcnt(0)\ns_branch .Loc_4\n.Lof_2:\ns_add_u32 s24, s0, .Lox_2-.Lobase\ns_addc_u32 s25, s1, 0\ns_waitcnt vmcnt(0)\n"
"s_branch .Loc_3\n.Lof_1:\ns_add_u32 s24, s0, .Lox_1-.Lobase\ns_addc_u32 s25, s1, 0\ns_waitcnt vmcnt(0)\ns_branch .Loc_2\n.Lof_0:\n"
"s_add_u32 s24, s0, .Lox_0-.Lobase\ns_addc_u32 s25, s1, 0\ns_waitcnt vmcnt(0)\n.Loc_1:\ns_bitcmp1_b32 s47, 1\ns_cbranch_scc0 .Loc_2\n"
"v_bitop3_b32 v69, v69, v71, s40 bitop3:0x7e\nv_bitop3_b32 v71, v37, v39, s40 bitop3:0x7e\nv_or_b32 v69, v69, v71\nv_cmp_ne_u32 vcc, 0, v69\n"
"v_mov_b32 v37, v68\nv_mov_b32 v39, v70\ns_cbranch_vccnz .Loc_2\ns_bitset0_b32 s3, 1\n.Loc_2:\ns_bitcmp1_b32 s47, 2\ns_cbranch_scc0 .Loc_3\n"
"v_bitop3_b32 v73, v73, v75, s40 bitop3:0x7e\nv_bitop3_b32 v75, v41, v43, s40 bitop3:0x7e\nv_or_b32 v73, v73, v75\nv_cmp_ne_u32 vcc, 0, v73\n"
"v_mov_b32 v41, v72\nv_mov_b32 v43, v74\ns_cbranch_vccnz .Loc_3\ns_bitset0_b32 s3, 2\n.Loc_3:\ns_bitcmp1_b32 s47, 3\ns_cbranch_scc0 .Loc_4\n"
"v_bitop3_b32 v77, v77, v79, s40 bitop3:0x7e\nv_bitop3_b32 v79, v45, v47, s40 bitop3:0x7e\nv_or_b32 v77, v77, v79\nv_cmp_ne_u32 vcc, 0, v77\n"
"v_mov_b32 v45, v76\nv_mov_b32 v47, v78\ns_cbranch_vccnz .Loc_4\ns_bitset0_b32 s3, 3\n.Loc_4:\ns_bitcmp1_b32 s47, 4\ns_cbranch_scc0 .Loc_5\n"
"v_bitop3_b32 v65, v65, v67, s40 bitop3:0x7e\nv_bitop3_b32 v67, v49, v51, s40 bitop3:0x7e\nv_or_b32 v65, v65, v67\nv_cmp_ne_u32 vcc, 0, v65\n"
"v_mov_b32 v49, v64\nv_mov_b32 v51, v66\ns_cbranch_vccnz .Loc_5\ns_bitset0_b32 s3, 4\n.Loc_5:\ns_bitcmp1_b32 s47, 5\ns_cbranch_scc0 .Loc_6\n"
"v_bitop3_b32 v69, v69, v71, s40 bitop3:0x7e\nv_bitop3_b32 v71, v53, v55, s40 bitop3:0x7e\nv_or_b32 v69, v69, v71\nv_cmp_ne_u32 vcc, 0, v69\n"
"v_mov_b32 v53, v68\nv_mov_b32 v55, v70\ns_cbranch_vccnz .Loc_6\ns_bitset0_b32 s3, 5\n.Loc_6:\ns_bitcmp1_b32 s47, 6\ns_cbranch_scc0 .Loc_7\n"
"v_bitop3_b32 v73, v73, v75, s40 bitop3:0x7e\nv_bitop3_b32 v75, v57, v59, s40 bitop3:0x7e\nv_or_b32 v73, v73, v75\nv_cmp_ne_u32 vcc, 0, v73\n"
"v_mov_b32 v57, v72\nv_mov_b32 v59, v74\ns_cbranch_vccnz .Loc_7\ns_bitset0_b32 s3, 6\n.Loc_7:\ns_bitcmp1_b32 s47, 7\ns_cbranch_scc0 .Loc_8\n"
"v_bitop3_b32 v77, v77, v79, s40 bitop3:0x7e\nv_bitop3_b32 v79, v61, v63, s40 bitop3:0x7e\nv_or_b32 v77, v77, v79\nv_cmp_ne_u32 vcc, 0, v77\n"
"v_mov_b32 v61, v76\nv_mov_b32 v63, v78\ns_cbranch_vccnz .Loc_8\ns_bitset0_b32 s3, 7\n.Loc_8:\ns_and_b32 s49, s3, 15\ns_lshl_b32 s49, s49, 4\n"
"s_andn2_b32 s47, s3, s49\ns_bitcmp1_b32 s47, 0\ns_cbranch_scc0 .Lol_0\nbuffer_load_dwordx4 v[32:35], v87, s[12:15], s20 offen offset:0 sc1\n"
"buffer_load_dwordx4 v[64:67], v87, s[12:15], s20 offen offset:1024 sc1\n.Lol_0:\ns_bitcmp1_b32 s47, 1\ns_cbranch_scc0 .Lol_1\n"
"buffer_load_dwordx4 v[36:39], v87, s[12:15], s20 offen offset:2048 sc1\nbuffer_load_dwordx4 v[68:71], v87, s[12:15], s20 offen offset:3072 sc1\n"
".Lol_1:\ns_bitcmp1_b32 s47, 2\ns_cbranch_scc0 .Lol_2\nbuffer_load_dwordx4 v[40:43], v87, s[12:15], s21 offen offset:0 sc1\n"
"buffer_load_dwordx4 v[72:75], v87, s[12:15], s21 offen offset:1024 sc1\n.Lol_2:\ns_bitcmp1_b32 s47, 3\ns_cbranch_scc0 .Lol_3\n"
"buffer_load_dwordx4 v[44:47], v87, s[12:15], s21 offen offset:2048 sc1\nbuffer_load_dwordx4 v[76:79], v87, s[12:15], s21 offen offset:3072 sc1\n"
".Lol_3:\ns_bitcmp1_b32 s47, 4\ns_cbranch_scc0 .Lol_4\nbuffer_load_dwordx4 v[48:51], v87, s[12:15], s22 offen offset:0 sc1\n"
"buffer_load_dwordx4 v[64:67], v87, s[12:15], s22 offen offset:1024 sc1\n.Lol_4:\ns_bitcmp1_b32 s47, 5\ns_cbranch_scc0 .Lol_5\n"
"buffer_load_dwordx4 v[52:55], v87, s[12:15], s22 offen offset:2048 sc1\nbuffer_load_dwordx4 v[68:71], v87, s[12:15], s22 offen offset:3072 sc1\n"
".Lol_5:\ns_bitcmp1_b32 s47, 6\ns_cbranch_scc0 .Lol_6\nbuffer_load_dwordx4 v[56:59], v87, s[12:15], s23 offen offset:0 sc1\n"
"buffer_load_dwordx4 v[72:75], v87, s[12:15], s23 offen offset:1024 sc1\n.Lol_6:\ns_bitcmp1_b32 s47, 7\ns_cbranch_scc0 .Lol_7\n"
"buffer_load_dwordx4 v[60:63], v87, s[12:15], s23 offen offset:2048 sc1\nbuffer_load_dwordx4 v[76:79], v87, s[12:15], s23 offen offset:3072 sc1\n"
".Lol_7:\ns_add_u32 s48, s48, 1\ns_cmp_lt_u32 s48, 0x40000\ns_waitcnt vmcnt(0) lgkmcnt(0)\ns_cbranch_scc0 .Lort_r\ns_setpc_b64 s[24:25]\n.Lsl1:\n"
"s_mov_b32 s48, 0\n.Lsp1:\nbuffer_load_dwordx2 v[76:77], v78, s[12:15], 0 offen sc1\ns_waitcnt vmcnt(0)\nv_cmp_eq_u64 vcc, s[40:41], v[76:77]\n"
"s_add_u32 s48, s48, 1\ns_cmp_lt_u32 s48, 0x40000\ns_cselect_b32 s100, vcc_lo, -1\ns_lshr_b32 s47, s100, 8\ns_and_b32 s100, s100, s47\n"
"s_bitcmp1_b32 s100, s3\ns_cbranch_scc0 .Lsp1\ns_branch .Lsr1\n.Lsl2:\ns_mov_b32 s48, 0\n.Lsp2:\n"
"buffer_load_dwordx2 v[76:77], v78, s[12:15], 0 offen sc1\ns_waitcnt vmcnt(0)\nv_cmp_eq_u64 vcc, s[40:41], v[76:77]\ns_add_u32 s48, s48, 1\n"
"s_cmp_lt_u32 s48, 0x40000\ns_cselect_b32 s100, vcc_lo, -1\ns_lshr_b32 s47, s100, 8\ns_and_b32 s100, s100, s47\ns_bitcmp1_b32 s100, s3\n"
"s_cbranch_scc0 .Lsp2\ns_branch .Lsr2\n.Lsl3:\ns_mov_b32 s48, 0\n.Lsp3:\nbuffer_load_dwordx2 v[76:77], v78, s[12:15], 0 offen sc1\ns_waitcnt vmcnt(0)\n"
"v_cmp_eq_u64 vcc, s[40:41], v[76:77]\ns_add_u32 s48, s48, 1\ns_cmp_lt_u32 s48, 0x40000\ns_cselect_b32 s100, vcc_lo, -1\ns_lshr_b32 s47, s100, 8\n"
"s_and_b32 s100, s100, s47\ns_bitcmp1_b32 s100, s3\ns_cbranch_scc0 .Lsp3\ns_branch .Lsr3\n.Lsl4:\ns_mov_b32 s48, 0\n.Lsp4:\n"
"buffer_load_dwordx2 v[76:77], v78, s[12:15], 0 offen sc1\ns_waitcnt vmcnt(0)\nv_cmp_eq_u64 vcc, s[40:41], v[76:77]\ns_add_u32 s48, s48, 1\n"
"s_cmp_lt_u32 s48, 0x40000\ns_cselect_b32 s100, vcc_lo, -1\ns_lshr_b32 s47, s100, 8\ns_and_b32 s100, s100, s47\ns_bitcmp1_b32 s100, s3\n"
"s_cbranch_scc0 .Lsp4\ns_branch .Lsr4\n.Lsl5:\ns_mov_b32 s48, 0\n.Lsp5:\nbuffer_load_dwordx2 v[76:77], v78, s[12:15], 0 offen sc1\ns_waitcnt vmcnt(0)\n"
"v_cmp_eq_u64 vcc, s[40:41], v[76:77]\ns_add_u32 s48, s48, 1\ns_cmp_lt_u32 s48, 0x40000\ns_cselect_b32 s100, vcc_lo, -1\ns_lshr_b32 s47, s100, 8\n"
"s_and_b32 s100, s100, s47\ns_bitcmp1_b32 s100, s3\ns_cbranch_scc0 .Lsp5\ns_branch .Lsr5\n.Lsl6:\ns_mov_b32 s48, 0\n.Lsp6:\n"
"buffer_load_dwordx2 v[76:77], v78, s[12:15], 0 offen sc1\ns_waitcnt vmcnt(0)\nv_cmp_eq_u64 vcc, s[40:41], v[76:77]\ns_add_u32 s48, s48, 1\n"
"s_cmp_lt_u32 s48, 0x40000\ns_cselect_b32 s100, vcc_lo, -1\ns_lshr_b32 s47, s100, 8\ns_and_b32 s100, s100, s47\ns_bitcmp1_b32 s100, s3\n"
"s_cbranch_scc0 .Lsp6\ns_branch .Lsr6\n.Lsl7:\ns_mov_b32 s48, 0\n.Lsp7:\nbuffer_load_dwordx2 v[76:77], v78, s[12:15], 0 offen sc1\ns_waitcnt vmcnt(0)\n"
"v_cmp_eq_u64 vcc, s[40:41], v[76:77]\ns_add_u32 s48, s48, 1\ns_cmp_lt_u32 s48, 0x40000\ns_cselect_b32 s100, vcc_lo, -1\ns_lshr_b32 s47, s100, 8\n"
"s_and_b32 s100, s100, s47\ns_bitcmp1_b32 s100, s3\ns_cbranch_scc0 .Lsp7\ns_branch .Lsr7\n.Lend:\n"
      : "+{s[0:1]}"(kp), "+{s2}"(bid), "+{v0}"(tid)
      :
      : "memory", "scc", "vcc", "v1", "v2", "v3", "v4", "v5", "v6", "v7", "v8", "v9", "v10", "v11", "v12", "v13", "v14", "v15", "v16",
        "v17", "v18", "v19", "v20", "v21", "v22", "v23", "v24", "v25", "v26", "v27", "v28", "v29", "v30", "v31", "v32",
        "v33", "v34", "v35", "v36", "v37", "v38", "v39", "v40", "v41", "v42", "v43", "v44", "v45", "v46", "v47", "v48",
        "v49", "v50", "v51", "v52", "v53", "v54", "v55", "v56", "v57", "v58", "v59", "v60", "v61", "v62", "v63", "v64",
        "v65", "v66", "v67", "v68", "v69", "v70", "v71", "v72", "v73", "v74", "v75", "v76", "v77", "v78", "v79", "v80",
        "v81", "v82", "v83", "v84", "v85", "v86", "v87", "v88", "v89", "v90", "v91", "v92", "v93", "v94", "v95", "v96",
        "v97", "v98", "v99", "v100", "v101", "v102", "v103", "v104", "v105", "v106", "v107", "v108", "v109", "v110", "v111", "v112",
        "v113", "v114", "v115", "v116", "v117", "v118", "v119", "v120", "v121", "v122", "v123", "v124", "v125", "v126", "v127", "v128",
        "v129", "v130", "v131", "v132", "v133", "v134", "v135", "v136", "v137", "v138", "v139", "v140", "v141", "v142", "v143", "v144",
        "v145", "v146", "v147", "v148", "v149", "v150", "v151", "v152", "v153", "v154", "v155", "v156", "v157", "v158", "v159", "v160",
        "v161", "v162", "v163", "v164", "v165", "v166", "v167", "v168", "v169", "v170", "v171", "v172", "v173", "v174", "v175", "v176",
        "v177", "v178", "v179", "v180", "v181", "v182", "v183", "v184", "v185", "v186", "v187", "v188", "v189", "v190", "v191", "v192",
        "v193", "v194", "v195", "v196", "v197", "v198", "v199", "v200", "v201", "v202", "v203", "v204", "v205", "v206", "v207", "v208",
        "v209", "v210", "v211", "v212", "v213", "v214", "v215", "v216", "v217", "v218", "v219", "v220", "v221", "v222", "v223", "v224",
        "v225", "v226", "v227", "v228", "v229", "v230", "v231", "v232", "v233", "v234", "v235", "v236", "v237", "v238", "v239", "v240",
        "v241", "v242", "v243", "v244", "v245", "v246", "v247", "v248", "v249", "v250", "v251", "v252", "v253", "v254", "v255", "s3",
        "s4", "s5", "s6", "s7", "s8", "s9", "s10", "s11", "s12", "s13", "s14", "s15", "s16", "s17", "s18", "s19",
        "s20", "s21", "s22", "s23", "s24", "s25", "s26", "s27", "s28", "s29", "s30", "s31", "s32", "s33", "s34", "s35",
        "s36", "s37", "s38", "s39", "s40", "s41", "s42", "s43", "s44", "s45", "s46", "s47", "s48", "s49", "s50", "s51",
        "s52", "s53", "s54", "s55", "s56", "s57", "s58", "s59", "s60", "s61", "s62", "s63", "s64", "s65", "s66", "s67",
        "s68", "s69", "s70", "s71", "s72", "s73", "s74", "s75", "s76", "s77", "s78", "s79", "s80", "s81", "s82", "s83",
        "s84", "s85", "s86", "s87", "s88", "s89", "s90", "s91", "s92", "s93", "s94", "s95", "s96", "s97", "s98", "s99",
        "s100", "s101");
}

extern "C" void kernel_launch(void* const* d_in, const int* in_sizes, int n_in, void* d_out, int out_size, void* d_ws,
                              size_t ws_size, hipStream_t stream) {
  constexpr u32 C = 0x00020000u;
  Args a = {{{d_in[0], 2048u * 512u * 2u, C}, {d_in[1], 512u * 1536u * 2u, C}, {(void*)(((uintptr_t)d_ws + 0x1fffffu) & ~(uintptr_t)0x1fffffu), 46137344u, C}, {d_out, 2048u * 512u * 2u, C},
             {d_in[2], 512u * 512u * 2u, C}, {d_in[4], 512u * 1792u * 2u, C}, {d_in[6], 1792u * 512u * 2u, C},
             {d_in[5], 1792u * 2u, C}, {d_in[7], 512u * 2u, C}}};
  constexpr int L = 163840;
  (void)hipFuncSetAttribute((const void*)fused, hipFuncAttributeMaxDynamicSharedMemorySize, L);
  fused<<<256, 512, L, stream>>>(a);
}
```
